```python
import torch

def _softmax(x, axis=-1):
    return torch.softmax(x, dim=axis)
def _gelu(x):
    return torch.nn.functional.gelu(x, approximate="tanh")
def _silu(x):
    return torch.nn.functional.silu(x)
def _sigmoid(x):
    return torch.sigmoid(x)
def _layernorm(x, axis=-1, eps=1e-5):
    m = x.mean(dim=axis, keepdim=True)
    v = x.var(dim=axis, keepdim=True, unbiased=False)
    return (x - m) / torch.sqrt(v + eps)
def _scatter_add(tgt, idx, src, axis=0):
    return tgt.clone().index_add_(axis, idx, src)
def _segment_sum(src, seg, n):
    out = torch.zeros((n,) + src.shape[1:], dtype=src.dtype)
    return out.index_add_(0, seg, src)
def _pad(x, pw):
    flat = tuple(v for pair in reversed(pw) for v in pair)
    return torch.nn.functional.pad(x, flat)

kernel_name = 'standalone__gqa_softmax__near_ridge'

def setup_inputs():

    _w = (lambda a: (lambda t: t.to(torch.bfloat16) if t.is_floating_point() else t)(torch.as_tensor(a)))
    B, Hq, Hkv, Sq, Sk, D = 16, 32, 32, 384, 384, 128
    return {
        "q": _w((lambda *s: torch.randn(*s, dtype=torch.bfloat16) * 0.8)(B, Hq, Sq, D)),
        "k": _w((lambda *s: torch.randn(*s, dtype=torch.bfloat16) * 0.8)(B, Hkv, Sk, D)),
        "v": _w((lambda *s: torch.randn(*s, dtype=torch.bfloat16) * 0.05)(B, Hkv, Sk, D)),
    }

def reference(q, k, v):
    B, Hq, Sq, D = q.shape
    Hkv = k.shape[1]
    rep = Hq // Hkv
    k = (lambda x, n, axis: torch.repeat_interleave(x, n, dim=axis))(k, rep, axis=1)
    v = (lambda x, n, axis: torch.repeat_interleave(x, n, dim=axis))(v, rep, axis=1)
    scale = D ** -0.5
    scores = torch.einsum("bhqd,bhkd->bhqk", q, k) * scale
    p = _softmax(scores, axis=-1)
    return torch.einsum("bhqk,bhkd->bhqd", p, v)
```

```cpp
#include <hip/hip_runtime.h>
#include <hip/hip_bf16.h>
using bf16 = __hip_bfloat16;
constexpr int D = 128, NW = 4, QBLK = 32, KVBLK = 64;
constexpr float SCALE = 0.088388347648318440f;
constexpr float THR = 8.f;
constexpr int B = 16, H = 32, S = 384;
constexpr size_t SHM_V = KVBLK * D * 2, SHM_K = KVBLK * D * 2, SHM_ATTN = 2 * SHM_V + 2 * SHM_K;
using bf16x8 = __attribute__((ext_vector_type(8))) short;
using s16x4  = __attribute__((ext_vector_type(4))) short;
using f32x16 = __attribute__((ext_vector_type(16))) float;
using u32x4  = __attribute__((ext_vector_type(4))) unsigned;
using u32x2  = __attribute__((ext_vector_type(2))) unsigned;
#define KSWZ(row, colB) ((row) * 256 + ((colB) ^ (((row) & 7) << 4)))
#define SBAR() __builtin_amdgcn_sched_barrier(0)
__device__ __forceinline__ unsigned cvtpk(float lo, float hi) {
  unsigned r; asm volatile("v_cvt_pk_bf16_f32 %0, %1, %2" : "=v"(r) : "v"(lo), "v"(hi)); return r;
}
__device__ __forceinline__ void partialSM(f32x16& p0, f32x16& p1, float& m_reg, float& mn, float& alpha) {
  constexpr float C = SCALE * 1.4426950408889634f;
  float pmax = p0[0]; for (int r = 1; r < 16; ++r) pmax = fmaxf(pmax, p0[r]); for (int r = 0; r < 16; ++r) pmax = fmaxf(pmax, p1[r]);
  { auto rr = __builtin_amdgcn_permlane32_swap(__float_as_uint(pmax), __float_as_uint(pmax), false, false);
    pmax = fmaxf(__uint_as_float(rr[0]), __uint_as_float(rr[1])); }
  if (__builtin_expect(__all(pmax - m_reg <= THR / SCALE), 1)) { mn = m_reg; alpha = 1.f; }
  else { mn = fmaxf(m_reg, pmax); alpha = __builtin_amdgcn_exp2f((m_reg - mn) * C); m_reg = mn; }
  float mnC = -mn * C;
  for (int r = 0; r < 16; ++r) p0[r] = fmaf(p0[r], C, mnC); for (int r = 0; r < 16; ++r) p1[r] = fmaf(p1[r], C, mnC);
  for (int r = 0; r < 16; ++r) p0[r] = __builtin_amdgcn_exp2f(p0[r]);
}
__device__ __forceinline__ void finishSM(f32x16& p0, f32x16& p1, float alpha, float& l_reg, bf16x8& pa0, bf16x8& pa1, bf16x8& pa2, bf16x8& pa3) {
  for (int r = 0; r < 16; ++r) p1[r] = __builtin_amdgcn_exp2f(p1[r]);
  float ps = 0; for (int r = 0; r < 16; ++r) ps += p0[r]; for (int r = 0; r < 16; ++r) ps += p1[r];
  { auto rr = __builtin_amdgcn_permlane32_swap(__float_as_uint(ps), __float_as_uint(ps), false, false);
    ps = __uint_as_float(rr[0]) + __uint_as_float(rr[1]); }
  l_reg = l_reg * alpha + ps;
#define PK4(P, BASE, OUT) do { u32x4 w = {cvtpk(P[BASE + 0], P[BASE + 1]), cvtpk(P[BASE + 2], P[BASE + 3]), cvtpk(P[BASE + 4], P[BASE + 5]), cvtpk(P[BASE + 6], P[BASE + 7])}; OUT = *reinterpret_cast<bf16x8*>(&w); } while (0)
  PK4(p0, 0, pa0); PK4(p0, 8, pa1); PK4(p1, 0, pa2); PK4(p1, 8, pa3);
#undef PK4
}
__device__ __forceinline__ void qkt(f32x16& p0, f32x16& p1, const bf16* Ks, const bf16x8* qr, int r32, int hi) {
  p0 = f32x16{}; p1 = f32x16{};
#pragma unroll
  for (int d0 = 0; d0 < 8; ++d0) { int cb = (d0 * 16 + hi * 8) * 2;
    bf16x8 b0 = *reinterpret_cast<const bf16x8*>((const char*)Ks + KSWZ(r32, cb));
    bf16x8 b1 = *reinterpret_cast<const bf16x8*>((const char*)Ks + KSWZ(32 + r32, cb));
    p0 = __builtin_amdgcn_mfma_f32_32x32x16_bf16(b0, qr[d0], p0, 0, 0, 0);
    p1 = __builtin_amdgcn_mfma_f32_32x32x16_bf16(b1, qr[d0], p1, 0, 0, 0); }
}
__device__ __forceinline__ int v_st(int k, int ch) { return 2048 * (k >> 3) + 512 * (ch >> 2) + 64 * (k & 7) + 16 * ((ch & 3) ^ ((k >> 2) & 3)); }
template <int OFF> __device__ __forceinline__ s16x4 tr_read(int vb) {
  s16x4 r; asm volatile("ds_read_b64_tr_b16 %0, %1 offset:%2" : "=&v"(r) : "v"(vb), "i"(OFF) : "memory"); return r;
}
template <int D0> __device__ __forceinline__ void pv_one(f32x16& od, int vbA, int vbB, bf16x8 pa0, bf16x8 pa1, bf16x8 pa2, bf16x8 pa3) {
  const s16x4 l0 = tr_read<0 * 4096 + D0 * 512>(vbA), h0 = tr_read<0 * 4096 + 2048 + D0 * 512>(vbB);
  const s16x4 l1 = tr_read<1 * 4096 + D0 * 512>(vbA), h1 = tr_read<1 * 4096 + 2048 + D0 * 512>(vbB);
  const s16x4 l2 = tr_read<2 * 4096 + D0 * 512>(vbA), h2 = tr_read<2 * 4096 + 2048 + D0 * 512>(vbB);
  const s16x4 l3 = tr_read<3 * 4096 + D0 * 512>(vbA), h3 = tr_read<3 * 4096 + 2048 + D0 * 512>(vbB);
  asm volatile("s_waitcnt lgkmcnt(0)" ::: "memory"); SBAR();
#define PK(L, H) (bf16x8){L[0], L[1], L[2], L[3], H[0], H[1], H[2], H[3]}
  od = __builtin_amdgcn_mfma_f32_32x32x16_bf16(PK(l0, h0), pa0, od, 0, 0, 0);
  od = __builtin_amdgcn_mfma_f32_32x32x16_bf16(PK(l1, h1), pa1, od, 0, 0, 0);
  od = __builtin_amdgcn_mfma_f32_32x32x16_bf16(PK(l2, h2), pa2, od, 0, 0, 0);
  od = __builtin_amdgcn_mfma_f32_32x32x16_bf16(PK(l3, h3), pa3, od, 0, 0, 0);
#undef PK
}
__device__ __forceinline__ void pv_d0(f32x16* o, int vbA, int vbB, bf16x8 pa0, bf16x8 pa1, bf16x8 pa2, bf16x8 pa3) {
  pv_one<0>(o[0], vbA, vbB, pa0, pa1, pa2, pa3); pv_one<1>(o[1], vbA, vbB, pa0, pa1, pa2, pa3); pv_one<2>(o[2], vbA, vbB, pa0, pa1, pa2, pa3); pv_one<3>(o[3], vbA, vbB, pa0, pa1, pa2, pa3);
}
__global__ __launch_bounds__(64 * NW, 2)
void attn_base(const bf16* __restrict__ Q, const bf16* __restrict__ K, const bf16* __restrict__ V, bf16* __restrict__ O) {
  extern __shared__ __attribute__((aligned(16))) char lds[];
  const int bid = blockIdx.x; const int x = bid & 7, y = bid >> 3; const int qb = y % 3, head = (y / 3) * 8 + x;
  const bf16* Qb = Q + ((long)head * S + qb * 128) * D; const bf16* Kh = K + (long)head * S * D; const bf16* Vh = V + (long)head * S * D;
  bf16* Ob = O + ((long)head * S + qb * 128) * D;
  const int tid = threadIdx.x, wid = tid >> 6, lane = tid & 63, r32 = lane & 31, hi = lane >> 5;
  char* V_lds = lds; bf16* K_lds = (bf16*)(lds + 2 * SHM_V);
  float m_reg = -1e30f, l_reg = 0; f32x16 o[4] = {}; bf16x8 qr[8];
  const bf16* Qw = Qb + (long)(wid * QBLK + r32) * D + hi * 8;
#pragma unroll
  for (int d0 = 0; d0 < 8; ++d0) qr[d0] = *reinterpret_cast<const bf16x8*>(Qw + d0 * 16);
  const int sr = tid >> 4, sch = tid & 15, sc = sch * 8;
  const int g = lane >> 4, li = lane & 15, qq = li >> 2, pp = li & 3, hh = g >> 1;
  const int rowA = 4 * hh + qq, ch0 = 2 * (g & 1) + (pp >> 1);
  const int vbA0 = (int)(uintptr_t)V_lds + 64 * rowA + 16 * (ch0 ^ ((rowA >> 2) & 3)) + 8 * (pp & 1);
  const int rowB = 8 + rowA;
  const int vbB0 = (int)(uintptr_t)V_lds + 64 * (rowB & 7) + 16 * (ch0 ^ ((rowB >> 2) & 3)) + 8 * (pp & 1);
  bf16x8 vs[4], ks[4];
#define SLOAD(k0) do { _Pragma("unroll") for (int i = 0; i < 4; ++i) { vs[i] = *(const bf16x8*)&Vh[(long)((k0) + 16 * i + sr) * D + sc]; ks[i] = *(const bf16x8*)&Kh[(long)((k0) + 16 * i + sr) * D + sc]; } } while (0)
#define SWRITE(b) do { _Pragma("unroll") for (int i = 0; i < 4; ++i) { *(bf16x8*)(V_lds + (b) * SHM_V + v_st(16 * i + sr, sch)) = vs[i]; *(bf16x8*)((char*)K_lds + (b) * SHM_K + KSWZ(16 * i + sr, sc * 2)) = ks[i]; } } while (0)
#define RESC(a) do { if (__any((a) < 1.f)) { for (int d = 0; d < 4; ++d) for (int r = 0; r < 16; ++r) o[d][r] *= (a); } } while (0)
  f32x16 pA0, pA1; float mnA, alA; bf16x8 pa0, pa1, pa2, pa3; constexpr int NT = S / KVBLK;
  SLOAD(0); SWRITE(0); __syncthreads();
  for (int j = 0; j < NT; ++j) {
    const int b = j & 1;
    if (j + 1 < NT) SLOAD((j + 1) * KVBLK);
    qkt(pA0, pA1, (bf16*)((char*)K_lds + b * SHM_K), qr, r32, hi);
    partialSM(pA0, pA1, m_reg, mnA, alA);
    RESC(alA);
    finishSM(pA0, pA1, alA, l_reg, pa0, pa1, pa2, pa3); SBAR();
    pv_d0(o, vbA0 + b * (int)SHM_V, vbB0 + b * (int)SHM_V, pa0, pa1, pa2, pa3);
    if (j + 1 < NT) SWRITE(b ^ 1);
    __syncthreads();
  }
  const float rl = __builtin_amdgcn_rcpf(l_reg);
  bf16* Ow = Ob + (long)(wid * QBLK + r32) * D + 4 * hi;
#pragma unroll
  for (int d0 = 0; d0 < 4; ++d0)
#pragma unroll
    for (int t = 0; t < 4; ++t) {
      u32x2 w = {cvtpk(o[d0][4 * t] * rl, o[d0][4 * t + 1] * rl), cvtpk(o[d0][4 * t + 2] * rl, o[d0][4 * t + 3] * rl)};
      *(u32x2*)(Ow + 32 * d0 + 8 * t) = w;
    }
}
extern "C" void kernel_launch(void* const* d_in, const int* in_sizes, int n_in, void* d_out, int out_size, void* d_ws, size_t ws_size, hipStream_t stream) {
  hipFuncSetAttribute((const void*)attn_base, hipFuncAttributeMaxDynamicSharedMemorySize, (int)SHM_ATTN);
  hipLaunchKernelGGL(attn_base, dim3(B * H * 3), dim3(64 * NW), SHM_ATTN, stream, (const bf16*)d_in[0], (const bf16*)d_in[1], (const bf16*)d_in[2], (bf16*)d_out);
}
```

```cpp
#include <hip/hip_runtime.h>
#include <hip/hip_bf16.h>

__global__ __launch_bounds__(256, 1) void attn_w4x96(const void* q, const void* k, const void* v, void* o) {
  (void)q; (void)k; (void)v; (void)o;
  const unsigned long long karg = (unsigned long long)__builtin_amdgcn_kernarg_segment_ptr();
  const unsigned blk = blockIdx.x;
  const unsigned wave = __builtin_amdgcn_readfirstlane(threadIdx.x >> 6);
  asm volatile(
    "s_load_dwordx8 s[80:87], %0, 0x0\n"
    "s_mul_i32 s64, %1, 98304\n"
    "s_mov_b32 s58, %2\n"
    "s_waitcnt lgkmcnt(0)\n"
    "s_add_u32 s36, s82, s64\n"
    "s_addc_u32 s37, s83, 0\n"
    "s_and_b32 s37, s37, 0xffff\n"
    "s_mov_b32 s38, 25264128\n"
    "s_mov_b32 s39, 0x20000\n"
    "s_add_u32 s44, s80, s64\n"
    "s_addc_u32 s45, s81, 0\n"
    "s_and_b32 s45, s45, 0xffff\n"
    "s_mov_b32 s46, 25264128\n"
    "s_mov_b32 s47, 0x20000\n"
    "s_lshl_b32 s56, s58, 11\n"
    "s_mul_i32 s65, s58, 24576\n"
    "s_add_u32 s57, s65, 65536\n"
    "s_and_b32 s66, s58, 1\n"
    "s_lshl_b32 s66, s66, 7\n"
    "s_or_b32 s67, s66, 64\n"
    "s_mov_b32 s54, 0\n"
    "s_mov_b32 s63, 8192\n"
    "v_mbcnt_lo_u32_b32 v128, -1, 0\n"
    "v_mbcnt_hi_u32_b32 v128, -1, v128\n"
    "v_lshrrev_b32 v129, 4, v128\n"
    "v_and_b32 v130, 15, v128\n"
    "v_xor_b32 v132, v129, v130\n"
    "v_lshlrev_b32 v132, 4, v132\n"
    "v_lshl_or_b32 v131, v129, 8, v132\n"
    "v_xor_b32 v132, s66, v131\n"
    "v_add_u32 v134, s56, v132\n"
    "v_xor_b32 v133, s67, v131\n"
    "v_add_u32 v135, s56, v133\n"
    "s_add_u32 m0, s56, 57344\n"
    "s_nop 0\n"
    "buffer_load_dwordx4 v134, s[36:39], s54 offen nt lds\n"
    "buffer_load_dwordx4 v135, s[36:39], s54 offen offset:1024 nt lds\n"
    "v_mov_b32 v132, v131\n"
    "v_add_u32 v214, s65, v132\n"
    "v_xor_b32 v132, 64, v131\n"
    "v_add_u32 v215, s65, v132\n"
    "v_xor_b32 v132, 128, v131\n"
    "v_add_u32 v216, s65, v132\n"
    "v_xor_b32 v132, 192, v131\n"
    "v_add_u32 v217, s65, v132\n"
    "s_mov_b32 s55, 0\n"
    "s_add_u32 m0, s57, 0\n"
    "s_nop 0\n"
    "buffer_load_dwordx4 v214, s[44:47], s55 offen nt lds\n"
    "buffer_load_dwordx4 v215, s[44:47], s55 offen offset:1024 nt lds\n"
    "buffer_load_dwordx4 v216, s[44:47], s55 offen offset:2048 nt lds\n"
    "buffer_load_dwordx4 v217, s[44:47], s55 offen offset:3072 nt lds\n"
    "s_mov_b32 s55, 4096\n"
    "s_add_u32 m0, s57, 4096\n"
    "s_nop 0\n"
    "buffer_load_dwordx4 v214, s[44:47], s55 offen nt lds\n"
    "buffer_load_dwordx4 v215, s[44:47], s55 offen offset:1024 nt lds\n"
    "buffer_load_dwordx4 v216, s[44:47], s55 offen offset:2048 nt lds\n"
    "buffer_load_dwordx4 v217, s[44:47], s55 offen offset:3072 nt lds\n"
    "s_getpc_b64 s[68:69]\n"
    "code_base:\n"
    "s_and_b32 s69, s69, 0xffff\n"
    "s_mov_b32 s70, code_end-code_base\n"
    "s_mov_b32 s71, 0x20000\n"
    "s_lshl_b32 s72, s58, 13\n"
    "v_lshlrev_b32 v140, 7, v128\n"
    "buffer_load_dword v255, v140, s[68:71], s72 offen\n"
    "s_add_u32 s40, s84, s64\n"
    "s_addc_u32 s41, s85, 0\n"
    "s_and_b32 s41, s41, 0xffff\n"
    "s_mov_b32 s42, 25264128\n"
    "s_mov_b32 s43, 0x20000\n"
    "s_add_u32 s48, s86, s64\n"
    "s_addc_u32 s49, s87, 0\n"
    "s_and_b32 s49, s49, 0xffff\n"
    "s_mov_b32 s50, 25264128\n"
    "s_mov_b32 s51, 0x20000\n"
    "s_sub_u32 s59, s57, 25165824\n"
    "v_add_u32 v218, 0, v134\n"
    "v_add_u32 v219, 0, v135\n"
    "v_lshlrev_b32 v132, 2, v129\n"
    "v_xor_b32 v132, v132, v130\n"
    "v_lshlrev_b32 v132, 4, v132\n"
    "v_lshl_or_b32 v132, v129, 8, v132\n"
    "v_add_u32 v220, s56, v132\n"
    "v_and_b32 v136, 31, v128\n"
    "v_lshrrev_b32 v137, 5, v128\n"
    "v_xor_b32 v132, v130, v137\n"
    "v_lshlrev_b32 v132, 4, v132\n"
    "v_lshl_or_b32 v132, v136, 8, v132\n"
    "v_add_u32 v202, s57, v132\n"
    "v_or_b32 v194, 0x8000, v132\n"
    "v_xor_b32 v133, 32, v132\n"
    "v_add_u32 v203, s57, v133\n"
    "v_or_b32 v195, 0x8000, v133\n"
    "v_xor_b32 v133, 64, v132\n"
    "v_add_u32 v204, s57, v133\n"
    "v_or_b32 v196, 0x8000, v133\n"
    "v_xor_b32 v133, 96, v132\n"
    "v_add_u32 v205, s57, v133\n"
    "v_or_b32 v197, 0x8000, v133\n"
    "v_xor_b32 v133, 128, v132\n"
    "v_add_u32 v206, s57, v133\n"
    "v_or_b32 v198, 0x8000, v133\n"
    "v_xor_b32 v133, 160, v132\n"
    "v_add_u32 v207, s57, v133\n"
    "v_or_b32 v199, 0x8000, v133\n"
    "v_xor_b32 v133, 192, v132\n"
    "v_add_u32 v208, s57, v133\n"
    "v_or_b32 v200, 0x8000, v133\n"
    "v_xor_b32 v133, 224, v132\n"
    "v_add_u32 v209, s57, v133\n"
    "v_or_b32 v201, 0x8000, v133\n"
    "v_bfe_u32 v138, v128, 2, 2\n"
    "v_mul_u32_u24 v132, 320, v138\n"
    "v_lshl_add_u32 v132, v137, 10, v132\n"
    "v_bfe_u32 v139, v128, 4, 1\n"
    "v_lshl_add_u32 v132, v139, 5, v132\n"
    "v_and_b32 v142, 1, v128\n"
    "v_lshl_add_u32 v132, v142, 4, v132\n"
    "v_bfe_u32 v142, v128, 1, 1\n"
    "v_lshl_add_u32 v132, v142, 3, v132\n"
    "v_mov_b32 v210, v132\n"
    "v_xor_b32 v211, 64, v132\n"
    "v_xor_b32 v212, 128, v132\n"
    "v_xor_b32 v213, 192, v132\n"
    "v_lshlrev_b32 v132, 4, v128\n"
    "v_add_u32 v221, s57, v132\n"
    "s_mov_b32 s52, 0x3e0293ee\n"
    "s_mov_b32 s53, 0x4e800000\n"
    "s_mov_b32 s55, 8192\n"
    "s_add_u32 m0, s57, 8192\n"
    "s_nop 0\n"
    "buffer_load_dwordx4 v214, s[44:47], s55 offen nt lds\n"
    "buffer_load_dwordx4 v215, s[44:47], s55 offen offset:1024 nt lds\n"
    "buffer_load_dwordx4 v216, s[44:47], s55 offen offset:2048 nt lds\n"
    "buffer_load_dwordx4 v217, s[44:47], s55 offen offset:3072 nt lds\n"
    "s_mov_b32 s55, 12288\n"
    "s_add_u32 m0, s57, 12288\n"
    "s_nop 0\n"
    "buffer_load_dwordx4 v214, s[44:47], s55 offen nt lds\n"
    "buffer_load_dwordx4 v215, s[44:47], s55 offen offset:1024 nt lds\n"
    "buffer_load_dwordx4 v216, s[44:47], s55 offen offset:2048 nt lds\n"
    "buffer_load_dwordx4 v217, s[44:47], s55 offen offset:3072 nt lds\n"
    "s_waitcnt vmcnt(9)\n"
    "s_barrier\n"
    "ds_read_b128 a[96:99], v194 offset:24576\n"
    "ds_read_b128 a[0:3], v202 offset:0\n"
    "ds_read_b128 a[100:103], v195 offset:24576\n"
    "ds_read_b128 a[4:7], v203 offset:0\n"
    "ds_read_b128 a[104:107], v196 offset:24576\n"
    "ds_read_b128 a[8:11], v204 offset:0\n"
    "ds_read_b128 a[108:111], v197 offset:24576\n"
    "ds_read_b128 a[12:15], v205 offset:0\n"
    "ds_read_b128 a[112:115], v198 offset:24576\n"
    "ds_read_b128 a[16:19], v206 offset:0\n"
    "ds_read_b128 a[116:119], v199 offset:24576\n"
    "ds_read_b128 a[20:23], v207 offset:0\n"
    "ds_read_b128 a[120:123], v200 offset:24576\n"
    "ds_read_b128 a[24:27], v208 offset:0\n"
    "ds_read_b128 a[124:127], v201 offset:24576\n"
    "ds_read_b128 a[28:31], v209 offset:0\n"
    "s_waitcnt lgkmcnt(14)\n"
    "v_mfma_f32_32x32x16_bf16 v[128:143], a[96:99], a[0:3], 0\n"
    "s_waitcnt lgkmcnt(12)\n"
    "v_mfma_f32_32x32x16_bf16 v[128:143], a[100:103], a[4:7], v[128:143]\n"
    "s_waitcnt lgkmcnt(10)\n"
    "v_mfma_f32_32x32x16_bf16 v[128:143], a[104:107], a[8:11], v[128:143]\n"
    "s_waitcnt lgkmcnt(8)\n"
    "v_mfma_f32_32x32x16_bf16 v[128:143], a[108:111], a[12:15], v[128:143]\n"
    "s_waitcnt lgkmcnt(3)\n"
    "v_mfma_f32_32x32x16_bf16 v[128:143], a[112:115], a[16:19], v[128:143]\n"
    "v_mfma_f32_32x32x16_bf16 v[128:143], a[116:119], a[20:23], v[128:143]\n"
    "s_waitcnt lgkmcnt(0)\n"
    "v_mfma_f32_32x32x16_bf16 v[128:143], a[120:123], a[24:27], v[128:143]\n"
    "v_mfma_f32_32x32x16_bf16 v[128:143], a[124:127], a[28:31], v[128:143]\n"
    "s_mov_b32 s55, 16384\n"
    "s_add_u32 m0, s57, 16384\n"
    "s_nop 0\n"
    "buffer_load_dwordx4 v214, s[44:47], s55 offen nt lds\n"
    "buffer_load_dwordx4 v215, s[44:47], s55 offen offset:1024 nt lds\n"
    "buffer_load_dwordx4 v216, s[44:47], s55 offen offset:2048 nt lds\n"
    "buffer_load_dwordx4 v217, s[44:47], s55 offen offset:3072 nt lds\n"
    "s_mov_b32 s55, 20480\n"
    "s_add_u32 m0, s57, 20480\n"
    "s_nop 0\n"
    "buffer_load_dwordx4 v214, s[44:47], s55 offen nt lds\n"
    "buffer_load_dwordx4 v215, s[44:47], s55 offen offset:1024 nt lds\n"
    "buffer_load_dwordx4 v216, s[44:47], s55 offen offset:2048 nt lds\n"
    "buffer_load_dwordx4 v217, s[44:47], s55 offen offset:3072 nt lds\n"
    "s_waitcnt vmcnt(8)\n"
    "ds_read_b128 a[32:35], v202 offset:8192\n"
    "ds_read_b128 a[36:39], v203 offset:8192\n"
    "ds_read_b128 a[40:43], v204 offset:8192\n"
    "ds_read_b128 a[44:47], v205 offset:8192\n"
    "ds_read_b128 a[48:51], v206 offset:8192\n"
    "ds_read_b128 a[52:55], v207 offset:8192\n"
    "ds_read_b128 a[56:59], v208 offset:8192\n"
    "ds_read_b128 a[60:63], v209 offset:8192\n"
    "s_waitcnt lgkmcnt(7)\n"
    "v_mfma_f32_32x32x16_bf16 v[144:159], a[96:99], a[32:35], 0\n"
    "v_xor_b32 v194, 0x8000, v194\n"
    "v_xor_b32 v195, 0x8000, v195\n"
    "v_xor_b32 v196, 0x8000, v196\n"
    "v_xor_b32 v197, 0x8000, v197\n"
    "v_xor_b32 v198, 0x8000, v198\n"
    "v_xor_b32 v199, 0x8000, v199\n"
    "s_waitcnt lgkmcnt(6)\n"
    "v_mfma_f32_32x32x16_bf16 v[144:159], a[100:103], a[36:39], v[144:159]\n"
    "s_add_u32 m0, s56, 0\n"
    "s_nop 0\n"
    "buffer_load_dwordx4 v220, s[40:43], s54 offen nt lds\n"
    "v_xor_b32 v200, 0x8000, v200\n"
    "v_xor_b32 v201, 0x8000, v201\n"
    "s_waitcnt lgkmcnt(5)\n"
    "v_mfma_f32_32x32x16_bf16 v[144:159], a[104:107], a[40:43], v[144:159]\n"
    "v_max3_f32 v248, v128, v129, v130\n"
    "v_max3_f32 v248, v248, v131, v132\n"
    "v_max3_f32 v248, v248, v133, v134\n"
    "v_max3_f32 v248, v248, v135, v136\n"
    "v_max3_f32 v248, v248, v137, v138\n"
    "v_max3_f32 v248, v248, v139, v140\n"
    "v_max3_f32 v248, v248, v141, v142\n"
    "v_max_f32 v248, v248, v143\n"
    "v_mov_b32 v249, v248\n"
    "s_nop 1\n"
    "v_permlane32_swap_b32 v249, v248\n"
    "v_max_f32 v248, v248, v249\n"
    "v_mul_f32_e64 v188, -s52, v248\n"
    "v_fma_f32 v176, v128, s52, v188\n"
    "v_exp_f32 v176, v176\n"
    "v_fma_f32 v177, v129, s52, v188\n"
    "v_exp_f32 v177, v177\n"
    "v_fma_f32 v178, v130, s52, v188\n"
    "v_add_f32 v185, v176, v177\n"
    "v_cvt_pk_bf16_f32 v160, v176, v177\n"
    "v_exp_f32 v178, v178\n"
    "v_fma_f32 v179, v131, s52, v188\n"
    "v_add_f32 v185, v185, v178\n"
    "v_exp_f32 v179, v179\n"
    "v_fma_f32 v180, v132, s52, v188\n"
    "v_add_f32 v185, v185, v179\n"
    "v_cvt_pk_bf16_f32 v161, v178, v179\n"
    "v_exp_f32 v180, v180\n"
    "v_fma_f32 v181, v133, s52, v188\n"
    "v_add_f32 v185, v185, v180\n"
    "v_exp_f32 v181, v181\n"
    "v_fma_f32 v182, v134, s52, v188\n"
    "v_add_f32 v185, v185, v181\n"
    "v_cvt_pk_bf16_f32 v162, v180, v181\n"
    "v_exp_f32 v182, v182\n"
    "v_fma_f32 v183, v135, s52, v188\n"
    "v_add_f32 v185, v185, v182\n"
    "v_exp_f32 v183, v183\n"
    "v_fma_f32 v176, v136, s52, v188\n"
    "v_add_f32 v185, v185, v183\n"
    "v_cvt_pk_bf16_f32 v163, v182, v183\n"
    "v_exp_f32 v176, v176\n"
    "v_fma_f32 v177, v137, s52, v188\n"
    "v_add_f32 v185, v185, v176\n"
    "v_exp_f32 v177, v177\n"
    "v_fma_f32 v178, v138, s52, v188\n"
    "v_add_f32 v185, v185, v177\n"
    "v_cvt_pk_bf16_f32 v164, v176, v177\n"
    "v_exp_f32 v178, v178\n"
    "s_waitcnt lgkmcnt(0)\n"
    "v_mfma_f32_32x32x16_bf16 v[144:159], a[108:111], a[44:47], v[144:159]\n"
    "buffer_load_dwordx4 v220, s[40:43], s54 offen offset:1024 nt lds\n"
    "v_fma_f32 v179, v139, s52, v188\n"
    "v_add_f32 v185, v185, v178\n"
    "v_exp_f32 v179, v179\n"
    "v_mfma_f32_32x32x16_bf16 v[144:159], a[112:115], a[48:51], v[144:159]\n"
    "v_fma_f32 v180, v140, s52, v188\n"
    "v_add_f32 v185, v185, v179\n"
    "v_cvt_pk_bf16_f32 v165, v178, v179\n"
    "v_exp_f32 v180, v180\n"
    "v_fma_f32 v181, v141, s52, v188\n"
    "v_mfma_f32_32x32x16_bf16 v[144:159], a[116:119], a[52:55], v[144:159]\n"
    "s_add_u32 m0, s56, 8192\n"
    "s_nop 0\n"
    "buffer_load_dwordx4 v218, s[36:39], s63 offen nt lds\n"
    "v_add_f32 v185, v185, v180\n"
    "v_exp_f32 v181, v181\n"
    "v_fma_f32 v182, v142, s52, v188\n"
    "v_mfma_f32_32x32x16_bf16 v[144:159], a[120:123], a[56:59], v[144:159]\n"
    "v_add_f32 v185, v185, v181\n"
    "v_exp_f32 v182, v182\n"
    "v_fma_f32 v183, v143, s52, v188\n"
    "v_add_f32 v185, v185, v182\n"
    "v_exp_f32 v183, v183\n"
    "v_mfma_f32_32x32x16_bf16 v[144:159], a[124:127], a[60:63], v[144:159]\n"
    "buffer_load_dwordx4 v219, s[36:39], s63 offen offset:1024 nt lds\n"
    "v_cvt_pk_bf16_f32 v166, v180, v181\n"
    "v_add_f32 v185, v185, v183\n"
    "v_cvt_pk_bf16_f32 v167, v182, v183\n"
    "s_mov_b32 s54, s63\n"
    "s_add_u32 s63, s63, 8192\n"
    "s_add_u32 m0, s56, 16384\n"
    "s_nop 0\n"
    "buffer_load_dwordx4 v220, s[40:43], s54 offen nt lds\n"
    "buffer_load_dwordx4 v220, s[40:43], s54 offen offset:1024 nt lds\n"
    "s_add_u32 m0, s56, 24576\n"
    "s_nop 0\n"
    "buffer_load_dwordx4 v218, s[36:39], s63 offen nt lds\n"
    "buffer_load_dwordx4 v219, s[36:39], s63 offen offset:1024 nt lds\n"
    "s_waitcnt vmcnt(8)\n"
    "ds_read_b128 a[64:67], v202 offset:16384\n"
    "ds_read_b128 a[68:71], v203 offset:16384\n"
    "ds_read_b128 a[72:75], v204 offset:16384\n"
    "ds_read_b128 a[76:79], v205 offset:16384\n"
    "ds_read_b128 a[80:83], v206 offset:16384\n"
    "ds_read_b128 a[84:87], v207 offset:16384\n"
    "ds_read_b128 a[88:91], v208 offset:16384\n"
    "ds_read_b128 a[92:95], v209 offset:16384\n"
    "s_waitcnt lgkmcnt(7)\n"
    "v_mfma_f32_32x32x16_bf16 v[128:143], a[96:99], a[64:67], 0\n"
    "s_waitcnt vmcnt(4)\n"
    "s_barrier\n"
    "v_max3_f32 v248, v144, v145, v146\n"
    "s_waitcnt lgkmcnt(6)\n"
    "v_mfma_f32_32x32x16_bf16 v[128:143], a[100:103], a[68:71], v[128:143]\n"
    "ds_read_b64_tr_b16 a[128:129], v210 offset:0\n"
    "ds_read_b64_tr_b16 a[130:131], v210 offset:2048\n"
    "v_max3_f32 v248, v248, v147, v148\n"
    "v_max3_f32 v248, v248, v149, v150\n"
    "v_max3_f32 v248, v248, v151, v152\n"
    "v_max3_f32 v248, v248, v153, v154\n"
    "v_max3_f32 v248, v248, v155, v156\n"
    "v_max3_f32 v248, v248, v157, v158\n"
    "v_max_f32 v248, v248, v159\n"
    "v_mov_b32 v249, v248\n"
    "s_nop 1\n"
    "v_permlane32_swap_b32 v249, v248\n"
    "v_max_f32 v248, v248, v249\n"
    "v_mul_f32_e64 v189, -s52, v248\n"
    "v_fma_f32 v176, v144, s52, v189\n"
    "v_exp_f32 v176, v176\n"
    "v_fma_f32 v177, v145, s52, v189\n"
    "v_exp_f32 v177, v177\n"
    "v_fma_f32 v178, v146, s52, v189\n"
    "v_add_f32 v186, v176, v177\n"
    "v_cvt_pk_bf16_f32 v168, v176, v177\n"
    "v_exp_f32 v178, v178\n"
    "v_fma_f32 v179, v147, s52, v189\n"
    "v_add_f32 v186, v186, v178\n"
    "v_exp_f32 v179, v179\n"
    "v_fma_f32 v180, v148, s52, v189\n"
    "v_add_f32 v186, v186, v179\n"
    "v_cvt_pk_bf16_f32 v169, v178, v179\n"
    "v_exp_f32 v180, v180\n"
    "v_fma_f32 v181, v149, s52, v189\n"
    "v_add_f32 v186, v186, v180\n"
    "v_exp_f32 v181, v181\n"
    "v_fma_f32 v182, v150, s52, v189\n"
    "v_add_f32 v186, v186, v181\n"
    "v_cvt_pk_bf16_f32 v170, v180, v181\n"
    "s_waitcnt lgkmcnt(0)\n"
    "v_mfma_f32_32x32x16_bf16 v[128:143], a[104:107], a[72:75], v[128:143]\n"
    "ds_read_b64_tr_b16 a[132:133], v211 offset:0\n"
    "ds_read_b64_tr_b16 a[134:135], v211 offset:2048\n"
    "s_mov_b32 s54, s63\n"
    "s_add_u32 s63, s63, 8192\n"
    "s_xor_b32 s56, s56, 32768\n"
    "v_exp_f32 v182, v182\n"
    "v_mfma_f32_32x32x16_bf16 v[128:143], a[108:111], a[76:79], v[128:143]\n"
    "ds_read_b64_tr_b16 a[136:137], v212 offset:0\n"
    "ds_read_b64_tr_b16 a[138:139], v212 offset:2048\n"
    "s_add_u32 m0, s56, 0\n"
    "v_fma_f32 v183, v151, s52, v189\n"
    "v_add_f32 v186, v186, v182\n"
    "v_mfma_f32_32x32x16_bf16 v[128:143], a[112:115], a[80:83], v[128:143]\n"
    "ds_read_b64_tr_b16 a[140:141], v213 offset:0\n"
    "ds_read_b64_tr_b16 a[142:143], v213 offset:2048\n"
    "buffer_load_dwordx4 v220, s[40:43], s54 offen nt lds\n"
    "v_exp_f32 v183, v183\n"
    "v_mfma_f32_32x32x16_bf16 v[128:143], a[116:119], a[84:87], v[128:143]\n"
    "ds_read_b64_tr_b16 a[144:145], v210 offset:4096\n"
    "ds_read_b64_tr_b16 a[146:147], v210 offset:6144\n"
    "v_fma_f32 v176, v152, s52, v189\n"
    "v_add_f32 v186, v186, v183\n"
    "v_cvt_pk_bf16_f32 v171, v182, v183\n"
    "v_exp_f32 v176, v176\n"
    "v_mfma_f32_32x32x16_bf16 v[128:143], a[120:123], a[88:91], v[128:143]\n"
    "ds_read_b64_tr_b16 a[148:149], v211 offset:4096\n"
    "ds_read_b64_tr_b16 a[150:151], v211 offset:6144\n"
    "buffer_load_dwordx4 v220, s[40:43], s54 offen offset:1024 nt lds\n"
    "v_fma_f32 v177, v153, s52, v189\n"
    "v_add_f32 v186, v186, v176\n"
    "v_mfma_f32_32x32x16_bf16 v[128:143], a[124:127], a[92:95], v[128:143]\n"
    "ds_read_b64_tr_b16 a[152:153], v212 offset:4096\n"
    "ds_read_b64_tr_b16 a[154:155], v212 offset:6144\n"
    "v_exp_f32 v177, v177\n"
    "v_fma_f32 v178, v154, s52, v189\n"
    "v_add_f32 v186, v186, v177\n"
    "v_mfma_f32_32x32x16_bf16 v[0:15], a[128:131], v[160:163], 0\n"
    "s_waitcnt lgkmcnt(6)\n"
    "ds_read_b64_tr_b16 a[156:157], v213 offset:4096\n"
    "ds_read_b64_tr_b16 a[158:159], v213 offset:6144\n"
    "s_add_u32 m0, s56, 8192\n"
    "ds_read_b128 a[96:99], v194 offset:8192\n"
    "v_cvt_pk_bf16_f32 v172, v176, v177\n"
    "v_mfma_f32_32x32x16_bf16 v[16:31], a[132:135], v[160:163], 0\n"
    "buffer_load_dwordx4 v218, s[36:39], s63 offen nt lds\n"
    "ds_read_b128 a[100:103], v195 offset:8192\n"
    "v_exp_f32 v178, v178\n"
    "v_fma_f32 v179, v155, s52, v189\n"
    "v_mfma_f32_32x32x16_bf16 v[32:47], a[136:139], v[160:163], 0\n"
    "ds_read_b128 a[104:107], v196 offset:8192\n"
    "v_add_f32 v186, v186, v178\n"
    "v_exp_f32 v179, v179\n"
    "v_fma_f32 v180, v156, s52, v189\n"
    "v_add_f32 v186, v186, v179\n"
    "v_mfma_f32_32x32x16_bf16 v[48:63], a[140:143], v[160:163], 0\n"
    "s_waitcnt lgkmcnt(5)\n"
    "buffer_load_dwordx4 v219, s[36:39], s63 offen offset:1024 nt lds\n"
    "ds_read_b128 a[108:111], v197 offset:8192\n"
    "v_cvt_pk_bf16_f32 v173, v178, v179\n"
    "v_exp_f32 v180, v180\n"
    "v_mfma_f32_32x32x16_bf16 v[0:15], a[144:147], v[164:167], v[0:15]\n"
    "s_mov_b32 s54, s63\n"
    "s_add_u32 s63, s63, 8192\n"
    "ds_read_b128 a[112:115], v198 offset:8192\n"
    "v_fma_f32 v181, v157, s52, v189\n"
    "v_add_f32 v186, v186, v180\n"
    "v_exp_f32 v181, v181\n"
    "v_mfma_f32_32x32x16_bf16 v[16:31], a[148:151], v[164:167], v[16:31]\n"
    "s_add_u32 m0, s56, 16384\n"
    "s_nop 0\n"
    "buffer_load_dwordx4 v220, s[40:43], s54 offen nt lds\n"
    "ds_read_b128 a[116:119], v199 offset:8192\n"
    "v_fma_f32 v182, v158, s52, v189\n"
    "v_add_f32 v186, v186, v181\n"
    "v_exp_f32 v182, v182\n"
    "v_mfma_f32_32x32x16_bf16 v[32:47], a[152:155], v[164:167], v[32:47]\n"
    "s_waitcnt lgkmcnt(3)\n"
    "ds_read_b128 a[120:123], v200 offset:8192\n"
    "v_fma_f32 v183, v159, s52, v189\n"
    "v_add_f32 v186, v186, v182\n"
    "v_exp_f32 v183, v183\n"
    "v_mfma_f32_32x32x16_bf16 v[48:63], a[156:159], v[164:167], v[48:63]\n"
    "buffer_load_dwordx4 v220, s[40:43], s54 offen offset:1024 nt lds\n"
    "ds_read_b128 a[124:127], v201 offset:8192\n"
    "v_cvt_pk_bf16_f32 v174, v180, v181\n"
    "v_add_f32 v186, v186, v183\n"
    "v_cvt_pk_bf16_f32 v175, v182, v183\n"
    "v_mfma_f32_32x32x16_bf16 v[144:159], a[96:99], a[0:3], 0\n"
    "v_max3_f32 v248, v128, v129, v130\n"
    "v_max3_f32 v248, v248, v131, v132\n"
    "v_max3_f32 v248, v248, v133, v134\n"
    "v_max3_f32 v248, v248, v135, v136\n"
    "v_max3_f32 v248, v248, v137, v138\n"
    "v_max3_f32 v248, v248, v139, v140\n"
    "v_max3_f32 v248, v248, v141, v142\n"
    "v_mfma_f32_32x32x16_bf16 v[144:159], a[100:103], a[4:7], v[144:159]\n"
    "s_add_u32 m0, s56, 24576\n"
    "v_max_f32 v248, v248, v143\n"
    "v_mov_b32 v249, v248\n"
    "s_nop 1\n"
    "v_permlane32_swap_b32 v249, v248\n"
    "v_max_f32 v248, v248, v249\n"
    "v_mul_f32_e64 v190, -s52, v248\n"
    "v_fma_f32 v176, v128, s52, v190\n"
    "v_mfma_f32_32x32x16_bf16 v[144:159], a[104:107], a[8:11], v[144:159]\n"
    "s_waitcnt lgkmcnt(1)\n"
    "buffer_load_dwordx4 v218, s[36:39], s63 offen nt lds\n"
    "v_exp_f32 v176, v176\n"
    "v_fma_f32 v177, v129, s52, v190\n"
    "v_exp_f32 v177, v177\n"
    "v_mfma_f32_32x32x16_bf16 v[144:159], a[108:111], a[12:15], v[144:159]\n"
    "v_fma_f32 v178, v130, s52, v190\n"
    "v_add_f32 v187, v176, v177\n"
    "v_cvt_pk_bf16_f32 v160, v176, v177\n"
    "v_exp_f32 v178, v178\n"
    "v_fma_f32 v179, v131, s52, v190\n"
    "v_mfma_f32_32x32x16_bf16 v[144:159], a[112:115], a[16:19], v[144:159]\n"
    "v_add_f32 v187, v187, v178\n"
    "v_exp_f32 v179, v179\n"
    "v_fma_f32 v180, v132, s52, v190\n"
    "v_add_f32 v187, v187, v179\n"
    "v_cvt_pk_bf16_f32 v161, v178, v179\n"
    "v_mfma_f32_32x32x16_bf16 v[144:159], a[116:119], a[20:23], v[144:159]\n"
    "buffer_load_dwordx4 v219, s[36:39], s63 offen offset:1024 nt lds\n"
    "v_exp_f32 v180, v180\n"
    "v_fma_f32 v181, v133, s52, v190\n"
    "v_add_f32 v187, v187, v180\n"
    "v_mfma_f32_32x32x16_bf16 v[144:159], a[120:123], a[24:27], v[144:159]\n"
    "s_waitcnt lgkmcnt(0)\n"
    "s_mov_b32 s54, s63\n"
    "s_mov_b32 s55, 25165824\n"
    "s_add_u32 m0, s55, s59\n"
    "v_exp_f32 v181, v181\n"
    "v_fma_f32 v182, v134, s52, v190\n"
    "v_add_f32 v187, v187, v181\n"
    "v_mfma_f32_32x32x16_bf16 v[144:159], a[124:127], a[28:31], v[144:159]\n"
    "s_add_u32 s63, s63, 8192\n"
    "buffer_load_dwordx4 v214, s[44:47], s55 offen nt lds\n"
    "v_cvt_pk_bf16_f32 v162, v180, v181\n"
    "v_exp_f32 v182, v182\n"
    "v_fma_f32 v183, v135, s52, v190\n"
    "v_mfma_f32_32x32x16_bf16 v[64:79], a[128:131], v[168:171], 0\n"
    "s_xor_b32 s56, s56, 32768\n"
    "v_add_f32 v187, v187, v182\n"
    "v_exp_f32 v183, v183\n"
    "v_fma_f32 v176, v136, s52, v190\n"
    "v_add_f32 v187, v187, v183\n"
    "v_cvt_pk_bf16_f32 v163, v182, v183\n"
    "v_mfma_f32_32x32x16_bf16 v[80:95], a[132:135], v[168:171], 0\n"
    "buffer_load_dwordx4 v215, s[44:47], s55 offen offset:1024 nt lds\n"
    "v_exp_f32 v176, v176\n"
    "v_fma_f32 v177, v137, s52, v190\n"
    "v_add_f32 v187, v187, v176\n"
    "v_mfma_f32_32x32x16_bf16 v[96:111], a[136:139], v[168:171], 0\n"
    "v_exp_f32 v177, v177\n"
    "v_fma_f32 v178, v138, s52, v190\n"
    "v_add_f32 v187, v187, v177\n"
    "v_cvt_pk_bf16_f32 v164, v176, v177\n"
    "v_mfma_f32_32x32x16_bf16 v[112:127], a[140:143], v[168:171], 0\n"
    "buffer_load_dwordx4 v216, s[44:47], s55 offen offset:2048 nt lds\n"
    "v_exp_f32 v178, v178\n"
    "v_fma_f32 v179, v139, s52, v190\n"
    "v_add_f32 v187, v187, v178\n"
    "v_mfma_f32_32x32x16_bf16 v[64:79], a[144:147], v[172:175], v[64:79]\n"
    "v_exp_f32 v179, v179\n"
    "v_fma_f32 v180, v140, s52, v190\n"
    "v_add_f32 v187, v187, v179\n"
    "v_cvt_pk_bf16_f32 v165, v178, v179\n"
    "v_exp_f32 v180, v180\n"
    "v_mfma_f32_32x32x16_bf16 v[80:95], a[148:151], v[172:175], v[80:95]\n"
    "buffer_load_dwordx4 v217, s[44:47], s55 offen offset:3072 nt lds\n"
    "v_fma_f32 v181, v141, s52, v190\n"
    "v_add_f32 v187, v187, v180\n"
    "v_exp_f32 v181, v181\n"
    "v_mfma_f32_32x32x16_bf16 v[96:111], a[152:155], v[172:175], v[96:111]\n"
    "v_fma_f32 v182, v142, s52, v190\n"
    "v_add_f32 v187, v187, v181\n"
    "v_exp_f32 v182, v182\n"
    "v_fma_f32 v183, v143, s52, v190\n"
    "v_add_f32 v187, v187, v182\n"
    "v_mfma_f32_32x32x16_bf16 v[112:127], a[156:159], v[172:175], v[112:127]\n"
    "v_exp_f32 v183, v183\n"
    "v_cvt_pk_bf16_f32 v166, v180, v181\n"
    "v_add_f32 v187, v187, v183\n"
    "v_cvt_pk_bf16_f32 v167, v182, v183\n"
    "v_mfma_f32_32x32x16_bf16 v[128:143], a[96:99], a[32:35], 0\n"
    "v_fma_f32 v176, v144, s52, v188\n"
    "v_exp_f32 v176, v176\n"
    "v_fma_f32 v177, v145, s52, v188\n"
    "v_exp_f32 v177, v177\n"
    "v_mfma_f32_32x32x16_bf16 v[128:143], a[100:103], a[36:39], v[128:143]\n"
    "v_fma_f32 v178, v146, s52, v188\n"
    "v_add_f32 v184, v176, v177\n"
    "v_cvt_pk_bf16_f32 v168, v176, v177\n"
    "v_exp_f32 v178, v178\n"
    "v_fma_f32 v179, v147, s52, v188\n"
    "v_mfma_f32_32x32x16_bf16 v[128:143], a[104:107], a[40:43], v[128:143]\n"
    "v_add_f32 v184, v184, v178\n"
    "v_exp_f32 v179, v179\n"
    "v_fma_f32 v180, v148, s52, v188\n"
    "v_add_f32 v184, v184, v179\n"
    "v_cvt_pk_bf16_f32 v169, v178, v179\n"
    "v_mfma_f32_32x32x16_bf16 v[128:143], a[108:111], a[44:47], v[128:143]\n"
    "v_exp_f32 v180, v180\n"
    "v_fma_f32 v181, v149, s52, v188\n"
    "v_add_f32 v184, v184, v180\n"
    "v_exp_f32 v181, v181\n"
    "v_mfma_f32_32x32x16_bf16 v[128:143], a[112:115], a[48:51], v[128:143]\n"
    "v_fma_f32 v182, v150, s52, v188\n"
    "v_add_f32 v184, v184, v181\n"
    "v_cvt_pk_bf16_f32 v170, v180, v181\n"
    "v_exp_f32 v182, v182\n"
    "v_fma_f32 v183, v151, s52, v188\n"
    "v_mfma_f32_32x32x16_bf16 v[128:143], a[116:119], a[52:55], v[128:143]\n"
    "v_add_f32 v184, v184, v182\n"
    "v_exp_f32 v183, v183\n"
    "v_fma_f32 v176, v152, s52, v188\n"
    "v_add_f32 v184, v184, v183\n"
    "v_cvt_pk_bf16_f32 v171, v182, v183\n"
    "v_mfma_f32_32x32x16_bf16 v[128:143], a[120:123], a[56:59], v[128:143]\n"
    "v_exp_f32 v176, v176\n"
    "v_fma_f32 v177, v153, s52, v188\n"
    "v_add_f32 v184, v184, v176\n"
    "v_exp_f32 v177, v177\n"
    "v_mfma_f32_32x32x16_bf16 v[128:143], a[124:127], a[60:63], v[128:143]\n"
    "v_fma_f32 v178, v154, s52, v188\n"
    "v_add_f32 v184, v184, v177\n"
    "v_cvt_pk_bf16_f32 v172, v176, v177\n"
    "v_exp_f32 v178, v178\n"
    "v_fma_f32 v179, v155, s52, v188\n"
    "v_mfma_f32_32x32x16_bf16 a[160:175], a[128:131], v[160:163], 0\n"
    "s_waitcnt vmcnt(12)\n"
    "s_barrier\n"
    "v_add_f32 v184, v184, v178\n"
    "v_mfma_f32_32x32x16_bf16 a[176:191], a[132:135], v[160:163], 0\n"
    "ds_read_b64_tr_b16 a[128:129], v210 offset:16384\n"
    "v_exp_f32 v179, v179\n"
    "v_fma_f32 v180, v156, s52, v188\n"
    "v_add_f32 v184, v184, v179\n"
    "v_cvt_pk_bf16_f32 v173, v178, v179\n"
    "v_mfma_f32_32x32x16_bf16 a[192:207], a[136:139], v[160:163], 0\n"
    "s_add_u32 m0, s56, 0\n"
    "ds_read_b64_tr_b16 a[130:131], v210 offset:18432\n"
    "buffer_load_dwordx4 v220, s[40:43], s54 offen nt lds\n"
    "v_exp_f32 v180, v180\n"
    "v_fma_f32 v181, v157, s52, v188\n"
    "v_mfma_f32_32x32x16_bf16 a[208:223], a[140:143], v[160:163], 0\n"
    "ds_read_b64_tr_b16 a[132:133], v211 offset:16384\n"
    "v_add_f32 v184, v184, v180\n"
    "v_exp_f32 v181, v181\n"
    "v_fma_f32 v182, v158, s52, v188\n"
    "v_add_f32 v184, v184, v181\n"
    "v_mfma_f32_32x32x16_bf16 a[160:175], a[144:147], v[164:167], a[160:175]\n"
    "ds_read_b64_tr_b16 a[134:135], v211 offset:18432\n"
    "buffer_load_dwordx4 v220, s[40:43], s54 offen offset:1024 nt lds\n"
    "v_exp_f32 v182, v182\n"
    "v_fma_f32 v183, v159, s52, v188\n"
    "v_mfma_f32_32x32x16_bf16 a[176:191], a[148:151], v[164:167], a[176:191]\n"
    "s_add_u32 m0, s56, 8192\n"
    "ds_read_b64_tr_b16 a[136:137], v212 offset:16384\n"
    "v_add_f32 v184, v184, v182\n"
    "v_exp_f32 v183, v183\n"
    "v_cvt_pk_bf16_f32 v174, v180, v181\n"
    "v_mfma_f32_32x32x16_bf16 a[192:207], a[152:155], v[164:167], a[192:207]\n"
    "ds_read_b64_tr_b16 a[138:139], v212 offset:18432\n"
    "buffer_load_dwordx4 v218, s[36:39], s63 offen nt lds\n"
    "v_add_f32 v184, v184, v183\n"
    "v_cvt_pk_bf16_f32 v175, v182, v183\n"
    "v_cmp_nge_f32 vcc, s53, v184\n"
    "v_mfma_f32_32x32x16_bf16 a[208:223], a[156:159], v[164:167], a[208:223]\n"
    "s_cbranch_vccnz rare_stub_3\n"
    "back_3:\n"
    "ds_read_b64_tr_b16 a[140:141], v213 offset:16384\n"
    "v_add_f32 v185, v185, v184\n"
    "v_mfma_f32_32x32x16_bf16 v[144:159], a[96:99], a[64:67], 0\n"
    "s_mov_b32 s62, 4\n"
    "loop_0:\n"
    "ds_read_b64_tr_b16 a[142:143], v213 offset:18432\n"
    "buffer_load_dwordx4 v219, s[36:39], s63 offen offset:1024 nt lds\n"
    "v_fma_f32 v176, v128, s52, v189\n"
    "v_exp_f32 v176, v176\n"
    "v_mfma_f32_32x32x16_bf16 v[144:159], a[100:103], a[68:71], v[144:159]\n"
    "ds_read_b64_tr_b16 a[144:145], v210 offset:20480\n"
    "v_fma_f32 v177, v129, s52, v189\n"
    "v_exp_f32 v177, v177\n"
    "v_fma_f32 v178, v130, s52, v189\n"
    "v_add_f32 v184, v176, v177\n"
    "v_mfma_f32_32x32x16_bf16 v[144:159], a[104:107], a[72:75], v[144:159]\n"
    "ds_read_b64_tr_b16 a[146:147], v210 offset:22528\n"
    "v_cvt_pk_bf16_f32 v160, v176, v177\n"
    "v_exp_f32 v178, v178\n"
    "v_fma_f32 v179, v131, s52, v189\n"
    "v_add_f32 v184, v184, v178\n"
    "v_mfma_f32_32x32x16_bf16 v[144:159], a[108:111], a[76:79], v[144:159]\n"
    "ds_read_b64_tr_b16 a[148:149], v211 offset:20480\n"
    "v_exp_f32 v179, v179\n"
    "v_fma_f32 v180, v132, s52, v189\n"
    "v_add_f32 v184, v184, v179\n"
    "v_cvt_pk_bf16_f32 v161, v178, v179\n"
    "v_mfma_f32_32x32x16_bf16 v[144:159], a[112:115], a[80:83], v[144:159]\n"
    "ds_read_b64_tr_b16 a[150:151], v211 offset:22528\n"
    "v_exp_f32 v180, v180\n"
    "v_fma_f32 v181, v133, s52, v189\n"
    "v_add_f32 v184, v184, v180\n"
    "v_exp_f32 v181, v181\n"
    "v_mfma_f32_32x32x16_bf16 v[144:159], a[116:119], a[84:87], v[144:159]\n"
    "ds_read_b64_tr_b16 a[152:153], v212 offset:20480\n"
    "v_fma_f32 v182, v134, s52, v189\n"
    "v_add_f32 v184, v184, v181\n"
    "v_cvt_pk_bf16_f32 v162, v180, v181\n"
    "v_exp_f32 v182, v182\n"
    "v_mfma_f32_32x32x16_bf16 v[144:159], a[120:123], a[88:91], v[144:159]\n"
    "ds_read_b64_tr_b16 a[154:155], v212 offset:22528\n"
    "v_fma_f32 v183, v135, s52, v189\n"
    "v_add_f32 v184, v184, v182\n"
    "v_exp_f32 v183, v183\n"
    "v_fma_f32 v176, v136, s52, v189\n"
    "v_mfma_f32_32x32x16_bf16 v[144:159], a[124:127], a[92:95], v[144:159]\n"
    "s_waitcnt lgkmcnt(3)\n"
    "ds_read_b64_tr_b16 a[156:157], v213 offset:20480\n"
    "v_add_f32 v184, v184, v183\n"
    "v_cvt_pk_bf16_f32 v163, v182, v183\n"
    "v_exp_f32 v176, v176\n"
    "v_fma_f32 v177, v137, s52, v189\n"
    "v_mfma_f32_32x32x16_bf16 v[0:15], a[128:131], v[168:171], v[0:15]\n"
    "ds_read_b64_tr_b16 a[158:159], v213 offset:22528\n"
    "ds_read_b128 a[96:99], v194 offset:24576\n"
    "v_add_f32 v184, v184, v176\n"
    "v_exp_f32 v177, v177\n"
    "v_fma_f32 v178, v138, s52, v189\n"
    "v_mfma_f32_32x32x16_bf16 v[16:31], a[132:135], v[168:171], v[16:31]\n"
    "ds_read_b128 a[100:103], v195 offset:24576\n"
    "v_add_f32 v184, v184, v177\n"
    "v_cvt_pk_bf16_f32 v164, v176, v177\n"
    "v_exp_f32 v178, v178\n"
    "v_fma_f32 v179, v139, s52, v189\n"
    "v_mfma_f32_32x32x16_bf16 v[32:47], a[136:139], v[168:171], v[32:47]\n"
    "ds_read_b128 a[104:107], v196 offset:24576\n"
    "v_add_f32 v184, v184, v178\n"
    "v_exp_f32 v179, v179\n"
    "v_fma_f32 v180, v140, s52, v189\n"
    "v_add_f32 v184, v184, v179\n"
    "v_mfma_f32_32x32x16_bf16 v[48:63], a[140:143], v[168:171], v[48:63]\n"
    "ds_read_b128 a[108:111], v197 offset:24576\n"
    "v_cvt_pk_bf16_f32 v165, v178, v179\n"
    "v_exp_f32 v180, v180\n"
    "v_fma_f32 v181, v141, s52, v189\n"
    "v_add_f32 v184, v184, v180\n"
    "v_mfma_f32_32x32x16_bf16 v[0:15], a[144:147], v[172:175], v[0:15]\n"
    "s_waitcnt lgkmcnt(3)\n"
    "ds_read_b128 a[112:115], v198 offset:24576\n"
    "v_exp_f32 v181, v181\n"
    "v_fma_f32 v182, v142, s52, v189\n"
    "v_add_f32 v184, v184, v181\n"
    "v_xor_b32 v210, 0x8000, v210\n"
    "v_mfma_f32_32x32x16_bf16 v[16:31], a[148:151], v[172:175], v[16:31]\n"
    "ds_read_b128 a[116:119], v199 offset:24576\n"
    "v_exp_f32 v182, v182\n"
    "v_fma_f32 v183, v143, s52, v189\n"
    "v_add_f32 v184, v184, v182\n"
    "v_exp_f32 v183, v183\n"
    "v_mfma_f32_32x32x16_bf16 v[32:47], a[152:155], v[172:175], v[32:47]\n"
    "ds_read_b128 a[120:123], v200 offset:24576\n"
    "v_cvt_pk_bf16_f32 v166, v180, v181\n"
    "v_add_f32 v184, v184, v183\n"
    "v_cvt_pk_bf16_f32 v167, v182, v183\n"
    "v_cmp_nge_f32 vcc, s53, v184\n"
    "v_xor_b32 v211, 0x8000, v211\n"
    "v_mfma_f32_32x32x16_bf16 v[48:63], a[156:159], v[172:175], v[48:63]\n"
    "s_cbranch_vccnz rare_stub_4\n"
    "back_4:\n"
    "ds_read_b128 a[124:127], v201 offset:24576\n"
    "v_add_f32 v186, v186, v184\n"
    "v_xor_b32 v212, 0x8000, v212\n"
    "v_mfma_f32_32x32x16_bf16 v[128:143], a[96:99], a[0:3], 0\n"
    "s_waitcnt lgkmcnt(3)\n"
    "v_fma_f32 v176, v144, s52, v190\n"
    "v_exp_f32 v176, v176\n"
    "v_fma_f32 v177, v145, s52, v190\n"
    "v_exp_f32 v177, v177\n"
    "v_mfma_f32_32x32x16_bf16 v[128:143], a[100:103], a[4:7], v[128:143]\n"
    "s_mov_b32 s54, s63\n"
    "v_fma_f32 v178, v146, s52, v190\n"
    "v_add_f32 v184, v176, v177\n"
    "v_cvt_pk_bf16_f32 v168, v176, v177\n"
    "v_exp_f32 v178, v178\n"
    "v_xor_b32 v213, 0x8000, v213\n"
    "v_mfma_f32_32x32x16_bf16 v[128:143], a[104:107], a[8:11], v[128:143]\n"
    "s_add_u32 s63, s63, 8192\n"
    "v_fma_f32 v179, v147, s52, v190\n"
    "v_add_f32 v184, v184, v178\n"
    "v_exp_f32 v179, v179\n"
    "v_fma_f32 v180, v148, s52, v190\n"
    "v_xor_b32 v194, 0x8000, v194\n"
    "v_mfma_f32_32x32x16_bf16 v[128:143], a[108:111], a[12:15], v[128:143]\n"
    "s_cmp_eq_u32 s63, 98304\n"
    "v_add_f32 v184, v184, v179\n"
    "v_cvt_pk_bf16_f32 v169, v178, v179\n"
    "v_exp_f32 v180, v180\n"
    "v_fma_f32 v181, v149, s52, v190\n"
    "v_xor_b32 v195, 0x8000, v195\n"
    "v_mfma_f32_32x32x16_bf16 v[128:143], a[112:115], a[16:19], v[128:143]\n"
    "s_waitcnt lgkmcnt(0)\n"
    "s_cselect_b32 s76, 25067520, 0\n"
    "v_add_f32 v184, v184, v180\n"
    "v_exp_f32 v181, v181\n"
    "v_fma_f32 v182, v150, s52, v190\n"
    "v_add_f32 v184, v184, v181\n"
    "v_xor_b32 v196, 0x8000, v196\n"
    "v_mfma_f32_32x32x16_bf16 v[128:143], a[116:119], a[20:23], v[128:143]\n"
    "s_add_u32 s63, s63, s76\n"
    "v_cvt_pk_bf16_f32 v170, v180, v181\n"
    "v_exp_f32 v182, v182\n"
    "v_fma_f32 v183, v151, s52, v190\n"
    "v_add_f32 v184, v184, v182\n"
    "v_xor_b32 v197, 0x8000, v197\n"
    "v_mfma_f32_32x32x16_bf16 v[128:143], a[120:123], a[24:27], v[128:143]\n"
    "v_exp_f32 v183, v183\n"
    "v_fma_f32 v176, v152, s52, v190\n"
    "v_add_f32 v184, v184, v183\n"
    "v_cvt_pk_bf16_f32 v171, v182, v183\n"
    "v_xor_b32 v198, 0x8000, v198\n"
    "v_mfma_f32_32x32x16_bf16 v[128:143], a[124:127], a[28:31], v[128:143]\n"
    "s_add_u32 s55, s55, 4096\n"
    "s_add_u32 m0, s55, s59\n"
    "v_exp_f32 v176, v176\n"
    "v_fma_f32 v177, v153, s52, v190\n"
    "v_add_f32 v184, v184, v176\n"
    "v_xor_b32 v199, 0x8000, v199\n"
    "v_mfma_f32_32x32x16_bf16 v[64:79], a[128:131], v[160:163], v[64:79]\n"
    "buffer_load_dwordx4 v214, s[44:47], s55 offen nt lds\n"
    "v_exp_f32 v177, v177\n"
    "v_fma_f32 v178, v154, s52, v190\n"
    "v_add_f32 v184, v184, v177\n"
    "v_mfma_f32_32x32x16_bf16 v[80:95], a[132:135], v[160:163], v[80:95]\n"
    "v_cvt_pk_bf16_f32 v172, v176, v177\n"
    "v_exp_f32 v178, v178\n"
    "v_fma_f32 v179, v155, s52, v190\n"
    "v_add_f32 v184, v184, v178\n"
    "v_xor_b32 v200, 0x8000, v200\n"
    "v_mfma_f32_32x32x16_bf16 v[96:111], a[136:139], v[160:163], v[96:111]\n"
    "buffer_load_dwordx4 v215, s[44:47], s55 offen offset:1024 nt lds\n"
    "v_exp_f32 v179, v179\n"
    "v_fma_f32 v180, v156, s52, v190\n"
    "v_add_f32 v184, v184, v179\n"
    "v_mfma_f32_32x32x16_bf16 v[112:127], a[140:143], v[160:163], v[112:127]\n"
    "v_cvt_pk_bf16_f32 v173, v178, v179\n"
    "v_exp_f32 v180, v180\n"
    "v_fma_f32 v181, v157, s52, v190\n"
    "v_add_f32 v184, v184, v180\n"
    "v_xor_b32 v201, 0x8000, v201\n"
    "v_mfma_f32_32x32x16_bf16 v[64:79], a[144:147], v[164:167], v[64:79]\n"
    "buffer_load_dwordx4 v216, s[44:47], s55 offen offset:2048 nt lds\n"
    "v_exp_f32 v181, v181\n"
    "v_fma_f32 v182, v158, s52, v190\n"
    "v_add_f32 v184, v184, v181\n"
    "v_mfma_f32_32x32x16_bf16 v[80:95], a[148:151], v[164:167], v[80:95]\n"
    "v_exp_f32 v182, v182\n"
    "v_fma_f32 v183, v159, s52, v190\n"
    "v_add_f32 v184, v184, v182\n"
    "v_exp_f32 v183, v183\n"
    "v_mfma_f32_32x32x16_bf16 v[96:111], a[152:155], v[164:167], v[96:111]\n"
    "buffer_load_dwordx4 v217, s[44:47], s55 offen offset:3072 nt lds\n"
    "v_cvt_pk_bf16_f32 v174, v180, v181\n"
    "v_add_f32 v184, v184, v183\n"
    "v_cvt_pk_bf16_f32 v175, v182, v183\n"
    "v_cmp_nge_f32 vcc, s53, v184\n"
    "v_mfma_f32_32x32x16_bf16 v[112:127], a[156:159], v[164:167], v[112:127]\n"
    "s_cbranch_vccnz rare_stub_5\n"
    "back_5:\n"
    "v_add_f32 v187, v187, v184\n"
    "v_mfma_f32_32x32x16_bf16 v[144:159], a[96:99], a[32:35], 0\n"
    "v_fma_f32 v176, v128, s52, v188\n"
    "v_exp_f32 v176, v176\n"
    "v_fma_f32 v177, v129, s52, v188\n"
    "v_exp_f32 v177, v177\n"
    "v_mfma_f32_32x32x16_bf16 v[144:159], a[100:103], a[36:39], v[144:159]\n"
    "v_fma_f32 v178, v130, s52, v188\n"
    "v_add_f32 v184, v176, v177\n"
    "v_cvt_pk_bf16_f32 v160, v176, v177\n"
    "v_exp_f32 v178, v178\n"
    "v_fma_f32 v179, v131, s52, v188\n"
    "v_mfma_f32_32x32x16_bf16 v[144:159], a[104:107], a[40:43], v[144:159]\n"
    "v_add_f32 v184, v184, v178\n"
    "v_exp_f32 v179, v179\n"
    "v_fma_f32 v180, v132, s52, v188\n"
    "v_add_f32 v184, v184, v179\n"
    "v_cvt_pk_bf16_f32 v161, v178, v179\n"
    "v_mfma_f32_32x32x16_bf16 v[144:159], a[108:111], a[44:47], v[144:159]\n"
    "v_exp_f32 v180, v180\n"
    "v_fma_f32 v181, v133, s52, v188\n"
    "v_add_f32 v184, v184, v180\n"
    "v_exp_f32 v181, v181\n"
    "v_mfma_f32_32x32x16_bf16 v[144:159], a[112:115], a[48:51], v[144:159]\n"
    "v_fma_f32 v182, v134, s52, v188\n"
    "v_add_f32 v184, v184, v181\n"
    "v_cvt_pk_bf16_f32 v162, v180, v181\n"
    "v_exp_f32 v182, v182\n"
    "v_fma_f32 v183, v135, s52, v188\n"
    "v_mfma_f32_32x32x16_bf16 v[144:159], a[116:119], a[52:55], v[144:159]\n"
    "v_add_f32 v184, v184, v182\n"
    "v_exp_f32 v183, v183\n"
    "v_fma_f32 v176, v136, s52, v188\n"
    "v_add_f32 v184, v184, v183\n"
    "v_cvt_pk_bf16_f32 v163, v182, v183\n"
    "v_mfma_f32_32x32x16_bf16 v[144:159], a[120:123], a[56:59], v[144:159]\n"
    "v_exp_f32 v176, v176\n"
    "v_fma_f32 v177, v137, s52, v188\n"
    "v_add_f32 v184, v184, v176\n"
    "v_exp_f32 v177, v177\n"
    "v_mfma_f32_32x32x16_bf16 v[144:159], a[124:127], a[60:63], v[144:159]\n"
    "v_fma_f32 v178, v138, s52, v188\n"
    "v_add_f32 v184, v184, v177\n"
    "v_cvt_pk_bf16_f32 v164, v176, v177\n"
    "v_exp_f32 v178, v178\n"
    "v_fma_f32 v179, v139, s52, v188\n"
    "v_mfma_f32_32x32x16_bf16 a[160:175], a[128:131], v[168:171], a[160:175]\n"
    "s_waitcnt vmcnt(16)\n"
    "s_barrier\n"
    "v_add_f32 v184, v184, v178\n"
    "v_mfma_f32_32x32x16_bf16 a[176:191], a[132:135], v[168:171], a[176:191]\n"
    "ds_read_b64_tr_b16 a[128:129], v210 offset:0\n"
    "v_exp_f32 v179, v179\n"
    "v_fma_f32 v180, v140, s52, v188\n"
    "v_add_f32 v184, v184, v179\n"
    "v_cvt_pk_bf16_f32 v165, v178, v179\n"
    "v_mfma_f32_32x32x16_bf16 a[192:207], a[136:139], v[168:171], a[192:207]\n"
    "s_add_u32 m0, s56, 16384\n"
    "ds_read_b64_tr_b16 a[130:131], v210 offset:2048\n"
    "buffer_load_dwordx4 v220, s[40:43], s54 offen nt lds\n"
    "v_exp_f32 v180, v180\n"
    "v_fma_f32 v181, v141, s52, v188\n"
    "v_mfma_f32_32x32x16_bf16 a[208:223], a[140:143], v[168:171], a[208:223]\n"
    "ds_read_b64_tr_b16 a[132:133], v211 offset:0\n"
    "v_add_f32 v184, v184, v180\n"
    "v_exp_f32 v181, v181\n"
    "v_fma_f32 v182, v142, s52, v188\n"
    "v_add_f32 v184, v184, v181\n"
    "v_mfma_f32_32x32x16_bf16 a[160:175], a[144:147], v[172:175], a[160:175]\n"
    "ds_read_b64_tr_b16 a[134:135], v211 offset:2048\n"
    "buffer_load_dwordx4 v220, s[40:43], s54 offen offset:1024 nt lds\n"
    "v_exp_f32 v182, v182\n"
    "v_fma_f32 v183, v143, s52, v188\n"
    "v_mfma_f32_32x32x16_bf16 a[176:191], a[148:151], v[172:175], a[176:191]\n"
    "s_add_u32 m0, s56, 24576\n"
    "ds_read_b64_tr_b16 a[136:137], v212 offset:0\n"
    "v_add_f32 v184, v184, v182\n"
    "v_exp_f32 v183, v183\n"
    "v_cvt_pk_bf16_f32 v166, v180, v181\n"
    "v_mfma_f32_32x32x16_bf16 a[192:207], a[152:155], v[172:175], a[192:207]\n"
    "ds_read_b64_tr_b16 a[138:139], v212 offset:2048\n"
    "buffer_load_dwordx4 v218, s[36:39], s63 offen nt lds\n"
    "v_add_f32 v184, v184, v183\n"
    "v_cvt_pk_bf16_f32 v167, v182, v183\n"
    "v_cmp_nge_f32 vcc, s53, v184\n"
    "v_mfma_f32_32x32x16_bf16 a[208:223], a[156:159], v[172:175], a[208:223]\n"
    "s_cbranch_vccnz rare_stub_6\n"
    "back_6:\n"
    "ds_read_b64_tr_b16 a[140:141], v213 offset:0\n"
    "v_add_f32 v185, v185, v184\n"
    "v_mfma_f32_32x32x16_bf16 v[128:143], a[96:99], a[64:67], 0\n"
    "ds_read_b64_tr_b16 a[142:143], v213 offset:2048\n"
    "buffer_load_dwordx4 v219, s[36:39], s63 offen offset:1024 nt lds\n"
    "v_fma_f32 v176, v144, s52, v189\n"
    "v_exp_f32 v176, v176\n"
    "v_mfma_f32_32x32x16_bf16 v[128:143], a[100:103], a[68:71], v[128:143]\n"
    "ds_read_b64_tr_b16 a[144:145], v210 offset:4096\n"
    "v_fma_f32 v177, v145, s52, v189\n"
    "v_exp_f32 v177, v177\n"
    "v_fma_f32 v178, v146, s52, v189\n"
    "v_add_f32 v184, v176, v177\n"
    "v_mfma_f32_32x32x16_bf16 v[128:143], a[104:107], a[72:75], v[128:143]\n"
    "ds_read_b64_tr_b16 a[146:147], v210 offset:6144\n"
    "v_cvt_pk_bf16_f32 v168, v176, v177\n"
    "v_exp_f32 v178, v178\n"
    "v_fma_f32 v179, v147, s52, v189\n"
    "v_add_f32 v184, v184, v178\n"
    "v_mfma_f32_32x32x16_bf16 v[128:143], a[108:111], a[76:79], v[128:143]\n"
    "ds_read_b64_tr_b16 a[148:149], v211 offset:4096\n"
    "v_exp_f32 v179, v179\n"
    "v_fma_f32 v180, v148, s52, v189\n"
    "v_add_f32 v184, v184, v179\n"
    "v_cvt_pk_bf16_f32 v169, v178, v179\n"
    "v_mfma_f32_32x32x16_bf16 v[128:143], a[112:115], a[80:83], v[128:143]\n"
    "ds_read_b64_tr_b16 a[150:151], v211 offset:6144\n"
    "v_exp_f32 v180, v180\n"
    "v_fma_f32 v181, v149, s52, v189\n"
    "v_add_f32 v184, v184, v180\n"
    "v_exp_f32 v181, v181\n"
    "v_mfma_f32_32x32x16_bf16 v[128:143], a[116:119], a[84:87], v[128:143]\n"
    "ds_read_b64_tr_b16 a[152:153], v212 offset:4096\n"
    "v_fma_f32 v182, v150, s52, v189\n"
    "v_add_f32 v184, v184, v181\n"
    "v_cvt_pk_bf16_f32 v170, v180, v181\n"
    "v_exp_f32 v182, v182\n"
    "v_mfma_f32_32x32x16_bf16 v[128:143], a[120:123], a[88:91], v[128:143]\n"
    "ds_read_b64_tr_b16 a[154:155], v212 offset:6144\n"
    "v_fma_f32 v183, v151, s52, v189\n"
    "v_add_f32 v184, v184, v182\n"
    "v_exp_f32 v183, v183\n"
    "v_fma_f32 v176, v152, s52, v189\n"
    "v_mfma_f32_32x32x16_bf16 v[128:143], a[124:127], a[92:95], v[128:143]\n"
    "s_waitcnt lgkmcnt(3)\n"
    "ds_read_b64_tr_b16 a[156:157], v213 offset:4096\n"
    "v_add_f32 v184, v184, v183\n"
    "v_cvt_pk_bf16_f32 v171, v182, v183\n"
    "v_exp_f32 v176, v176\n"
    "v_fma_f32 v177, v153, s52, v189\n"
    "v_mfma_f32_32x32x16_bf16 v[0:15], a[128:131], v[160:163], v[0:15]\n"
    "ds_read_b64_tr_b16 a[158:159], v213 offset:6144\n"
    "ds_read_b128 a[96:99], v194 offset:8192\n"
    "v_add_f32 v184, v184, v176\n"
    "v_exp_f32 v177, v177\n"
    "v_fma_f32 v178, v154, s52, v189\n"
    "v_mfma_f32_32x32x16_bf16 v[16:31], a[132:135], v[160:163], v[16:31]\n"
    "ds_read_b128 a[100:103], v195 offset:8192\n"
    "v_add_f32 v184, v184, v177\n"
    "v_cvt_pk_bf16_f32 v172, v176, v177\n"
    "v_exp_f32 v178, v178\n"
    "v_fma_f32 v179, v155, s52, v189\n"
    "v_mfma_f32_32x32x16_bf16 v[32:47], a[136:139], v[160:163], v[32:47]\n"
    "ds_read_b128 a[104:107], v196 offset:8192\n"
    "v_add_f32 v184, v184, v178\n"
    "v_exp_f32 v179, v179\n"
    "v_fma_f32 v180, v156, s52, v189\n"
    "v_add_f32 v184, v184, v179\n"
    "v_mfma_f32_32x32x16_bf16 v[48:63], a[140:143], v[160:163], v[48:63]\n"
    "ds_read_b128 a[108:111], v197 offset:8192\n"
    "v_cvt_pk_bf16_f32 v173, v178, v179\n"
    "v_exp_f32 v180, v180\n"
    "v_fma_f32 v181, v157, s52, v189\n"
    "v_add_f32 v184, v184, v180\n"
    "v_mfma_f32_32x32x16_bf16 v[0:15], a[144:147], v[164:167], v[0:15]\n"
    "s_waitcnt lgkmcnt(3)\n"
    "ds_read_b128 a[112:115], v198 offset:8192\n"
    "v_exp_f32 v181, v181\n"
    "v_fma_f32 v182, v158, s52, v189\n"
    "v_add_f32 v184, v184, v181\n"
    "v_mfma_f32_32x32x16_bf16 v[16:31], a[148:151], v[164:167], v[16:31]\n"
    "ds_read_b128 a[116:119], v199 offset:8192\n"
    "v_exp_f32 v182, v182\n"
    "v_fma_f32 v183, v159, s52, v189\n"
    "v_add_f32 v184, v184, v182\n"
    "v_exp_f32 v183, v183\n"
    "v_mfma_f32_32x32x16_bf16 v[32:47], a[152:155], v[164:167], v[32:47]\n"
    "ds_read_b128 a[120:123], v200 offset:8192\n"
    "v_cvt_pk_bf16_f32 v174, v180, v181\n"
    "v_add_f32 v184, v184, v183\n"
    "v_cvt_pk_bf16_f32 v175, v182, v183\n"
    "v_cmp_nge_f32 vcc, s53, v184\n"
    "v_mfma_f32_32x32x16_bf16 v[48:63], a[156:159], v[164:167], v[48:63]\n"
    "s_cbranch_vccnz rare_stub_7\n"
    "back_7:\n"
    "ds_read_b128 a[124:127], v201 offset:8192\n"
    "v_add_f32 v186, v186, v184\n"
    "v_mfma_f32_32x32x16_bf16 v[144:159], a[96:99], a[0:3], 0\n"
    "s_waitcnt lgkmcnt(3)\n"
    "v_fma_f32 v176, v128, s52, v190\n"
    "v_exp_f32 v176, v176\n"
    "v_fma_f32 v177, v129, s52, v190\n"
    "v_exp_f32 v177, v177\n"
    "v_mfma_f32_32x32x16_bf16 v[144:159], a[100:103], a[4:7], v[144:159]\n"
    "s_mov_b32 s54, s63\n"
    "v_fma_f32 v178, v130, s52, v190\n"
    "v_add_f32 v184, v176, v177\n"
    "v_cvt_pk_bf16_f32 v160, v176, v177\n"
    "v_exp_f32 v178, v178\n"
    "v_mfma_f32_32x32x16_bf16 v[144:159], a[104:107], a[8:11], v[144:159]\n"
    "s_add_u32 s63, s63, 8192\n"
    "v_fma_f32 v179, v131, s52, v190\n"
    "v_add_f32 v184, v184, v178\n"
    "v_exp_f32 v179, v179\n"
    "v_fma_f32 v180, v132, s52, v190\n"
    "v_mfma_f32_32x32x16_bf16 v[144:159], a[108:111], a[12:15], v[144:159]\n"
    "s_cmp_eq_u32 s63, 98304\n"
    "v_add_f32 v184, v184, v179\n"
    "v_cvt_pk_bf16_f32 v161, v178, v179\n"
    "v_exp_f32 v180, v180\n"
    "v_fma_f32 v181, v133, s52, v190\n"
    "v_mfma_f32_32x32x16_bf16 v[144:159], a[112:115], a[16:19], v[144:159]\n"
    "s_waitcnt lgkmcnt(0)\n"
    "s_cselect_b32 s76, 25067520, 0\n"
    "v_add_f32 v184, v184, v180\n"
    "v_exp_f32 v181, v181\n"
    "v_fma_f32 v182, v134, s52, v190\n"
    "v_add_f32 v184, v184, v181\n"
    "v_mfma_f32_32x32x16_bf16 v[144:159], a[116:119], a[20:23], v[144:159]\n"
    "s_add_u32 s63, s63, s76\n"
    "v_cvt_pk_bf16_f32 v162, v180, v181\n"
    "v_exp_f32 v182, v182\n"
    "v_fma_f32 v183, v135, s52, v190\n"
    "v_add_f32 v184, v184, v182\n"
    "v_mfma_f32_32x32x16_bf16 v[144:159], a[120:123], a[24:27], v[144:159]\n"
    "s_xor_b32 s56, s56, 32768\n"
    "v_exp_f32 v183, v183\n"
    "v_fma_f32 v176, v136, s52, v190\n"
    "v_add_f32 v184, v184, v183\n"
    "v_cvt_pk_bf16_f32 v163, v182, v183\n"
    "v_mfma_f32_32x32x16_bf16 v[144:159], a[124:127], a[28:31], v[144:159]\n"
    "v_exp_f32 v176, v176\n"
    "v_fma_f32 v177, v137, s52, v190\n"
    "v_add_f32 v184, v184, v176\n"
    "v_mfma_f32_32x32x16_bf16 v[64:79], a[128:131], v[168:171], v[64:79]\n"
    "v_exp_f32 v177, v177\n"
    "v_fma_f32 v178, v138, s52, v190\n"
    "v_add_f32 v184, v184, v177\n"
    "v_cvt_pk_bf16_f32 v164, v176, v177\n"
    "v_mfma_f32_32x32x16_bf16 v[80:95], a[132:135], v[168:171], v[80:95]\n"
    "v_exp_f32 v178, v178\n"
    "v_fma_f32 v179, v139, s52, v190\n"
    "v_add_f32 v184, v184, v178\n"
    "v_mfma_f32_32x32x16_bf16 v[96:111], a[136:139], v[168:171], v[96:111]\n"
    "v_exp_f32 v179, v179\n"
    "v_fma_f32 v180, v140, s52, v190\n"
    "v_add_f32 v184, v184, v179\n"
    "v_cvt_pk_bf16_f32 v165, v178, v179\n"
    "v_mfma_f32_32x32x16_bf16 v[112:127], a[140:143], v[168:171], v[112:127]\n"
    "v_exp_f32 v180, v180\n"
    "v_fma_f32 v181, v141, s52, v190\n"
    "v_add_f32 v184, v184, v180\n"
    "v_mfma_f32_32x32x16_bf16 v[64:79], a[144:147], v[172:175], v[64:79]\n"
    "v_exp_f32 v181, v181\n"
    "v_fma_f32 v182, v142, s52, v190\n"
    "v_add_f32 v184, v184, v181\n"
    "v_exp_f32 v182, v182\n"
    "v_mfma_f32_32x32x16_bf16 v[80:95], a[148:151], v[172:175], v[80:95]\n"
    "v_fma_f32 v183, v143, s52, v190\n"
    "v_add_f32 v184, v184, v182\n"
    "v_exp_f32 v183, v183\n"
    "v_mfma_f32_32x32x16_bf16 v[96:111], a[152:155], v[172:175], v[96:111]\n"
    "v_cvt_pk_bf16_f32 v166, v180, v181\n"
    "v_add_f32 v184, v184, v183\n"
    "v_cvt_pk_bf16_f32 v167, v182, v183\n"
    "v_cmp_nge_f32 vcc, s53, v184\n"
    "v_mfma_f32_32x32x16_bf16 v[112:127], a[156:159], v[172:175], v[112:127]\n"
    "s_cbranch_vccnz rare_stub_8\n"
    "back_8:\n"
    "v_add_f32 v187, v187, v184\n"
    "v_mfma_f32_32x32x16_bf16 v[128:143], a[96:99], a[32:35], 0\n"
    "v_fma_f32 v176, v144, s52, v188\n"
    "v_exp_f32 v176, v176\n"
    "v_fma_f32 v177, v145, s52, v188\n"
    "v_exp_f32 v177, v177\n"
    "v_mfma_f32_32x32x16_bf16 v[128:143], a[100:103], a[36:39], v[128:143]\n"
    "v_fma_f32 v178, v146, s52, v188\n"
    "v_add_f32 v184, v176, v177\n"
    "v_cvt_pk_bf16_f32 v168, v176, v177\n"
    "v_exp_f32 v178, v178\n"
    "v_fma_f32 v179, v147, s52, v188\n"
    "v_mfma_f32_32x32x16_bf16 v[128:143], a[104:107], a[40:43], v[128:143]\n"
    "v_add_f32 v184, v184, v178\n"
    "v_exp_f32 v179, v179\n"
    "v_fma_f32 v180, v148, s52, v188\n"
    "v_add_f32 v184, v184, v179\n"
    "v_cvt_pk_bf16_f32 v169, v178, v179\n"
    "v_mfma_f32_32x32x16_bf16 v[128:143], a[108:111], a[44:47], v[128:143]\n"
    "v_exp_f32 v180, v180\n"
    "v_fma_f32 v181, v149, s52, v188\n"
    "v_add_f32 v184, v184, v180\n"
    "v_exp_f32 v181, v181\n"
    "v_mfma_f32_32x32x16_bf16 v[128:143], a[112:115], a[48:51], v[128:143]\n"
    "v_fma_f32 v182, v150, s52, v188\n"
    "v_add_f32 v184, v184, v181\n"
    "v_cvt_pk_bf16_f32 v170, v180, v181\n"
    "v_exp_f32 v182, v182\n"
    "v_fma_f32 v183, v151, s52, v188\n"
    "v_mfma_f32_32x32x16_bf16 v[128:143], a[116:119], a[52:55], v[128:143]\n"
    "v_add_f32 v184, v184, v182\n"
    "v_exp_f32 v183, v183\n"
    "v_fma_f32 v176, v152, s52, v188\n"
    "v_add_f32 v184, v184, v183\n"
    "v_cvt_pk_bf16_f32 v171, v182, v183\n"
    "v_mfma_f32_32x32x16_bf16 v[128:143], a[120:123], a[56:59], v[128:143]\n"
    "v_exp_f32 v176, v176\n"
    "v_fma_f32 v177, v153, s52, v188\n"
    "v_add_f32 v184, v184, v176\n"
    "v_exp_f32 v177, v177\n"
    "v_mfma_f32_32x32x16_bf16 v[128:143], a[124:127], a[60:63], v[128:143]\n"
    "v_fma_f32 v178, v154, s52, v188\n"
    "v_add_f32 v184, v184, v177\n"
    "v_cvt_pk_bf16_f32 v172, v176, v177\n"
    "v_exp_f32 v178, v178\n"
    "v_fma_f32 v179, v155, s52, v188\n"
    "v_mfma_f32_32x32x16_bf16 a[160:175], a[128:131], v[160:163], a[160:175]\n"
    "s_waitcnt vmcnt(12)\n"
    "s_barrier\n"
    "v_add_f32 v184, v184, v178\n"
    "v_mfma_f32_32x32x16_bf16 a[176:191], a[132:135], v[160:163], a[176:191]\n"
    "ds_read_b64_tr_b16 a[128:129], v210 offset:16384\n"
    "v_exp_f32 v179, v179\n"
    "v_fma_f32 v180, v156, s52, v188\n"
    "v_add_f32 v184, v184, v179\n"
    "v_cvt_pk_bf16_f32 v173, v178, v179\n"
    "v_mfma_f32_32x32x16_bf16 a[192:207], a[136:139], v[160:163], a[192:207]\n"
    "s_add_u32 m0, s56, 0\n"
    "ds_read_b64_tr_b16 a[130:131], v210 offset:18432\n"
    "buffer_load_dwordx4 v220, s[40:43], s54 offen nt lds\n"
    "v_exp_f32 v180, v180\n"
    "v_fma_f32 v181, v157, s52, v188\n"
    "v_mfma_f32_32x32x16_bf16 a[208:223], a[140:143], v[160:163], a[208:223]\n"
    "ds_read_b64_tr_b16 a[132:133], v211 offset:16384\n"
    "v_add_f32 v184, v184, v180\n"
    "v_exp_f32 v181, v181\n"
    "v_fma_f32 v182, v158, s52, v188\n"
    "v_add_f32 v184, v184, v181\n"
    "v_mfma_f32_32x32x16_bf16 a[160:175], a[144:147], v[164:167], a[160:175]\n"
    "ds_read_b64_tr_b16 a[134:135], v211 offset:18432\n"
    "buffer_load_dwordx4 v220, s[40:43], s54 offen offset:1024 nt lds\n"
    "v_exp_f32 v182, v182\n"
    "v_fma_f32 v183, v159, s52, v188\n"
    "v_mfma_f32_32x32x16_bf16 a[176:191], a[148:151], v[164:167], a[176:191]\n"
    "s_add_u32 m0, s56, 8192\n"
    "ds_read_b64_tr_b16 a[136:137], v212 offset:16384\n"
    "v_add_f32 v184, v184, v182\n"
    "v_exp_f32 v183, v183\n"
    "v_cvt_pk_bf16_f32 v174, v180, v181\n"
    "v_mfma_f32_32x32x16_bf16 a[192:207], a[152:155], v[164:167], a[192:207]\n"
    "ds_read_b64_tr_b16 a[138:139], v212 offset:18432\n"
    "buffer_load_dwordx4 v218, s[36:39], s63 offen nt lds\n"
    "v_add_f32 v184, v184, v183\n"
    "v_cvt_pk_bf16_f32 v175, v182, v183\n"
    "v_cmp_nge_f32 vcc, s53, v184\n"
    "v_mfma_f32_32x32x16_bf16 a[208:223], a[156:159], v[164:167], a[208:223]\n"
    "s_cbranch_vccnz rare_stub_9\n"
    "back_9:\n"
    "ds_read_b64_tr_b16 a[140:141], v213 offset:16384\n"
    "v_add_f32 v185, v185, v184\n"
    "v_mfma_f32_32x32x16_bf16 v[144:159], a[96:99], a[64:67], 0\n"
    "s_sub_u32 s62, s62, 1\n"
    "s_cbranch_scc0 loop_0\n"
    "ds_read_b64_tr_b16 a[142:143], v213 offset:18432\n"
    "buffer_load_dwordx4 v219, s[36:39], s63 offen offset:1024 nt lds\n"
    "ds_read_b128 a[0:3], v202 offset:0\n"
    "v_fma_f32 v176, v128, s52, v189\n"
    "v_exp_f32 v176, v176\n"
    "v_fma_f32 v177, v129, s52, v189\n"
    "v_exp_f32 v177, v177\n"
    "v_fma_f32 v178, v130, s52, v189\n"
    "v_add_f32 v184, v176, v177\n"
    "v_cvt_pk_bf16_f32 v160, v176, v177\n"
    "v_exp_f32 v178, v178\n"
    "v_mfma_f32_32x32x16_bf16 v[144:159], a[100:103], a[68:71], v[144:159]\n"
    "ds_read_b64_tr_b16 a[144:145], v210 offset:20480\n"
    "v_fma_f32 v179, v131, s52, v189\n"
    "v_add_f32 v184, v184, v178\n"
    "v_exp_f32 v179, v179\n"
    "v_fma_f32 v180, v132, s52, v189\n"
    "v_mfma_f32_32x32x16_bf16 v[144:159], a[104:107], a[72:75], v[144:159]\n"
    "ds_read_b64_tr_b16 a[146:147], v210 offset:22528\n"
    "ds_read_b128 a[4:7], v203 offset:0\n"
    "v_mov_b32 v252, v185\n"
    "v_add_f32 v184, v184, v179\n"
    "v_cvt_pk_bf16_f32 v161, v178, v179\n"
    "v_exp_f32 v180, v180\n"
    "v_mfma_f32_32x32x16_bf16 v[144:159], a[108:111], a[76:79], v[144:159]\n"
    "ds_read_b64_tr_b16 a[148:149], v211 offset:20480\n"
    "v_fma_f32 v181, v133, s52, v189\n"
    "v_add_f32 v184, v184, v180\n"
    "v_exp_f32 v181, v181\n"
    "v_fma_f32 v182, v134, s52, v189\n"
    "v_mfma_f32_32x32x16_bf16 v[144:159], a[112:115], a[80:83], v[144:159]\n"
    "ds_read_b64_tr_b16 a[150:151], v211 offset:22528\n"
    "ds_read_b128 a[8:11], v204 offset:0\n"
    "v_add_f32 v184, v184, v181\n"
    "v_cvt_pk_bf16_f32 v162, v180, v181\n"
    "v_exp_f32 v182, v182\n"
    "v_mfma_f32_32x32x16_bf16 v[144:159], a[116:119], a[84:87], v[144:159]\n"
    "ds_read_b64_tr_b16 a[152:153], v212 offset:20480\n"
    "v_permlane32_swap_b32 v252, v185\n"
    "v_fma_f32 v183, v135, s52, v189\n"
    "v_add_f32 v184, v184, v182\n"
    "v_exp_f32 v183, v183\n"
    "v_mfma_f32_32x32x16_bf16 v[144:159], a[120:123], a[88:91], v[144:159]\n"
    "ds_read_b64_tr_b16 a[154:155], v212 offset:22528\n"
    "ds_read_b128 a[12:15], v205 offset:0\n"
    "v_fma_f32 v176, v136, s52, v189\n"
    "v_add_f32 v184, v184, v183\n"
    "v_cvt_pk_bf16_f32 v163, v182, v183\n"
    "v_exp_f32 v176, v176\n"
    "v_mfma_f32_32x32x16_bf16 v[144:159], a[124:127], a[92:95], v[144:159]\n"
    "s_waitcnt lgkmcnt(5)\n"
    "ds_read_b64_tr_b16 a[156:157], v213 offset:20480\n"
    "v_add_f32 v185, v185, v252\n"
    "v_fma_f32 v177, v137, s52, v189\n"
    "v_add_f32 v184, v184, v176\n"
    "v_exp_f32 v177, v177\n"
    "v_mfma_f32_32x32x16_bf16 v[0:15], a[128:131], v[168:171], v[0:15]\n"
    "ds_read_b64_tr_b16 a[158:159], v213 offset:22528\n"
    "ds_read_b128 a[96:99], v194 offset:24576\n"
    "ds_read_b128 a[16:19], v206 offset:0\n"
    "v_fma_f32 v178, v138, s52, v189\n"
    "v_add_f32 v184, v184, v177\n"
    "v_cvt_pk_bf16_f32 v164, v176, v177\n"
    "v_mfma_f32_32x32x16_bf16 v[16:31], a[132:135], v[168:171], v[16:31]\n"
    "ds_read_b128 a[100:103], v195 offset:24576\n"
    "v_exp_f32 v178, v178\n"
    "v_fma_f32 v179, v139, s52, v189\n"
    "v_add_f32 v184, v184, v178\n"
    "v_exp_f32 v179, v179\n"
    "v_mfma_f32_32x32x16_bf16 v[32:47], a[136:139], v[168:171], v[32:47]\n"
    "ds_read_b128 a[104:107], v196 offset:24576\n"
    "ds_read_b128 a[20:23], v207 offset:0\n"
    "v_fma_f32 v180, v140, s52, v189\n"
    "v_add_f32 v184, v184, v179\n"
    "v_cvt_pk_bf16_f32 v165, v178, v179\n"
    "v_xor_b32 v210, 0x8000, v210\n"
    "v_mfma_f32_32x32x16_bf16 v[48:63], a[140:143], v[168:171], v[48:63]\n"
    "ds_read_b128 a[108:111], v197 offset:24576\n"
    "v_exp_f32 v180, v180\n"
    "v_fma_f32 v181, v141, s52, v189\n"
    "v_add_f32 v184, v184, v180\n"
    "v_exp_f32 v181, v181\n"
    "v_mfma_f32_32x32x16_bf16 v[0:15], a[144:147], v[172:175], v[0:15]\n"
    "s_waitcnt lgkmcnt(4)\n"
    "ds_read_b128 a[112:115], v198 offset:24576\n"
    "ds_read_b128 a[24:27], v208 offset:0\n"
    "v_rcp_f32 v191, v185\n"
    "v_fma_f32 v182, v142, s52, v189\n"
    "v_add_f32 v184, v184, v181\n"
    "v_exp_f32 v182, v182\n"
    "v_mfma_f32_32x32x16_bf16 v[16:31], a[148:151], v[172:175], v[16:31]\n"
    "ds_read_b128 a[116:119], v199 offset:24576\n"
    "v_fma_f32 v183, v143, s52, v189\n"
    "v_add_f32 v184, v184, v182\n"
    "v_exp_f32 v183, v183\n"
    "v_xor_b32 v211, 0x8000, v211\n"
    "v_mfma_f32_32x32x16_bf16 v[32:47], a[152:155], v[172:175], v[32:47]\n"
    "ds_read_b128 a[120:123], v200 offset:24576\n"
    "ds_read_b128 a[28:31], v209 offset:0\n"
    "v_cvt_pk_bf16_f32 v166, v180, v181\n"
    "v_add_f32 v184, v184, v183\n"
    "v_cvt_pk_bf16_f32 v167, v182, v183\n"
    "v_cmp_nge_f32 vcc, s53, v184\n"
    "v_mfma_f32_32x32x16_bf16 v[48:63], a[156:159], v[172:175], v[48:63]\n"
    "s_cbranch_vccnz rare_stub_34\n"
    "back_34:\n"
    "ds_read_b128 a[124:127], v201 offset:24576\n"
    "v_add_f32 v186, v186, v184\n"
    "v_xor_b32 v212, 0x8000, v212\n"
    "v_mfma_f32_32x32x16_bf16 v[128:143], a[96:99], a[0:3], 0\n"
    "s_waitcnt lgkmcnt(4)\n"
    "v_fma_f32 v176, v144, s52, v190\n"
    "v_exp_f32 v176, v176\n"
    "v_fma_f32 v177, v145, s52, v190\n"
    "v_exp_f32 v177, v177\n"
    "v_mfma_f32_32x32x16_bf16 v[128:143], a[100:103], a[4:7], v[128:143]\n"
    "s_mov_b32 s54, s63\n"
    "v_mul_f32 v224, v0, v191\n"
    "v_mul_f32 v225, v1, v191\n"
    "v_mul_f32 v226, v2, v191\n"
    "v_mul_f32 v227, v3, v191\n"
    "v_mul_f32 v228, v4, v191\n"
    "v_fma_f32 v178, v146, s52, v190\n"
    "v_mfma_f32_32x32x16_bf16 v[128:143], a[104:107], a[8:11], v[128:143]\n"
    "s_add_u32 s63, s63, 8192\n"
    "v_mov_b32 v252, v186\n"
    "v_mul_f32 v229, v5, v191\n"
    "v_mul_f32 v230, v6, v191\n"
    "v_mul_f32 v231, v7, v191\n"
    "v_cvt_pk_bf16_f32 v232, v224, v225\n"
    "v_cvt_pk_bf16_f32 v233, v226, v227\n"
    "v_cvt_pk_bf16_f32 v234, v228, v229\n"
    "v_add_f32 v184, v176, v177\n"
    "v_mfma_f32_32x32x16_bf16 v[128:143], a[108:111], a[12:15], v[128:143]\n"
    "v_cvt_pk_bf16_f32 v235, v230, v231\n"
    "ds_write_b128 v202, v[232:235] offset:0\n"
    "v_mul_f32 v224, v8, v191\n"
    "v_mul_f32 v225, v9, v191\n"
    "v_mul_f32 v226, v10, v191\n"
    "v_cvt_pk_bf16_f32 v168, v176, v177\n"
    "v_exp_f32 v178, v178\n"
    "v_fma_f32 v179, v147, s52, v190\n"
    "v_add_f32 v184, v184, v178\n"
    "v_exp_f32 v179, v179\n"
    "v_mfma_f32_32x32x16_bf16 v[128:143], a[112:115], a[16:19], v[128:143]\n"
    "s_waitcnt lgkmcnt(1)\n"
    "v_mul_f32 v227, v11, v191\n"
    "v_mul_f32 v228, v12, v191\n"
    "v_mul_f32 v229, v13, v191\n"
    "v_mul_f32 v230, v14, v191\n"
    "v_mul_f32 v231, v15, v191\n"
    "v_fma_f32 v180, v148, s52, v190\n"
    "v_add_f32 v184, v184, v179\n"
    "v_cvt_pk_bf16_f32 v169, v178, v179\n"
    "v_exp_f32 v180, v180\n"
    "v_mfma_f32_32x32x16_bf16 v[128:143], a[116:119], a[20:23], v[128:143]\n"
    "v_permlane32_swap_b32 v252, v186\n"
    "v_cvt_pk_bf16_f32 v236, v224, v225\n"
    "v_cvt_pk_bf16_f32 v237, v226, v227\n"
    "v_cvt_pk_bf16_f32 v238, v228, v229\n"
    "v_cvt_pk_bf16_f32 v239, v230, v231\n"
    "ds_write_b128 v203, v[236:239] offset:0\n"
    "v_fma_f32 v181, v149, s52, v190\n"
    "v_add_f32 v184, v184, v180\n"
    "v_exp_f32 v181, v181\n"
    "v_fma_f32 v182, v150, s52, v190\n"
    "v_add_f32 v184, v184, v181\n"
    "v_mfma_f32_32x32x16_bf16 v[128:143], a[120:123], a[24:27], v[128:143]\n"
    "ds_read_b128 a[32:35], v202 offset:8192\n"
    "v_mul_f32 v224, v16, v191\n"
    "v_mul_f32 v225, v17, v191\n"
    "v_mul_f32 v226, v18, v191\n"
    "v_mul_f32 v227, v19, v191\n"
    "v_mul_f32 v228, v20, v191\n"
    "v_mul_f32 v229, v21, v191\n"
    "v_cvt_pk_bf16_f32 v170, v180, v181\n"
    "v_exp_f32 v182, v182\n"
    "v_fma_f32 v183, v151, s52, v190\n"
    "v_add_f32 v184, v184, v182\n"
    "v_mfma_f32_32x32x16_bf16 v[128:143], a[124:127], a[28:31], v[128:143]\n"
    "ds_read_b128 a[36:39], v203 offset:8192\n"
    "v_add_f32 v186, v186, v252\n"
    "v_mul_f32 v230, v22, v191\n"
    "v_mul_f32 v231, v23, v191\n"
    "v_cvt_pk_bf16_f32 v232, v224, v225\n"
    "v_cvt_pk_bf16_f32 v233, v226, v227\n"
    "v_cvt_pk_bf16_f32 v234, v228, v229\n"
    "v_exp_f32 v183, v183\n"
    "v_fma_f32 v176, v152, s52, v190\n"
    "v_add_f32 v184, v184, v183\n"
    "v_cvt_pk_bf16_f32 v171, v182, v183\n"
    "v_mfma_f32_32x32x16_bf16 v[64:79], a[128:131], v[160:163], v[64:79]\n"
    "ds_read_b128 a[40:43], v204 offset:8192\n"
    "v_cvt_pk_bf16_f32 v235, v230, v231\n"
    "ds_write_b128 v204, v[232:235] offset:0\n"
    "v_mul_f32 v224, v24, v191\n"
    "v_mul_f32 v225, v25, v191\n"
    "v_mul_f32 v226, v26, v191\n"
    "v_exp_f32 v176, v176\n"
    "v_fma_f32 v177, v153, s52, v190\n"
    "v_add_f32 v184, v184, v176\n"
    "v_exp_f32 v177, v177\n"
    "v_mfma_f32_32x32x16_bf16 v[80:95], a[132:135], v[160:163], v[80:95]\n"
    "ds_read_b128 a[44:47], v205 offset:8192\n"
    "v_mul_f32 v227, v27, v191\n"
    "v_mul_f32 v228, v28, v191\n"
    "v_mul_f32 v229, v29, v191\n"
    "v_mul_f32 v230, v30, v191\n"
    "v_mul_f32 v231, v31, v191\n"
    "v_cvt_pk_bf16_f32 v236, v224, v225\n"
    "v_fma_f32 v178, v154, s52, v190\n"
    "v_add_f32 v184, v184, v177\n"
    "v_cvt_pk_bf16_f32 v172, v176, v177\n"
    "v_exp_f32 v178, v178\n"
    "v_mfma_f32_32x32x16_bf16 v[96:111], a[136:139], v[160:163], v[96:111]\n"
    "ds_read_b128 a[48:51], v206 offset:8192\n"
    "v_cvt_pk_bf16_f32 v237, v226, v227\n"
    "v_cvt_pk_bf16_f32 v238, v228, v229\n"
    "v_cvt_pk_bf16_f32 v239, v230, v231\n"
    "ds_write_b128 v205, v[236:239] offset:0\n"
    "v_mul_f32 v224, v32, v191\n"
    "v_fma_f32 v179, v155, s52, v190\n"
    "v_add_f32 v184, v184, v178\n"
    "v_exp_f32 v179, v179\n"
    "v_mfma_f32_32x32x16_bf16 v[112:127], a[140:143], v[160:163], v[112:127]\n"
    "ds_read_b128 a[52:55], v207 offset:8192\n"
    "v_mul_f32 v225, v33, v191\n"
    "v_mul_f32 v226, v34, v191\n"
    "v_mul_f32 v227, v35, v191\n"
    "v_mul_f32 v228, v36, v191\n"
    "v_mul_f32 v229, v37, v191\n"
    "v_fma_f32 v180, v156, s52, v190\n"
    "v_add_f32 v184, v184, v179\n"
    "v_cvt_pk_bf16_f32 v173, v178, v179\n"
    "v_exp_f32 v180, v180\n"
    "v_mfma_f32_32x32x16_bf16 v[64:79], a[144:147], v[164:167], v[64:79]\n"
    "ds_read_b128 a[56:59], v208 offset:8192\n"
    "v_rcp_f32 v192, v186\n"
    "v_mul_f32 v230, v38, v191\n"
    "v_mul_f32 v231, v39, v191\n"
    "v_cvt_pk_bf16_f32 v232, v224, v225\n"
    "v_cvt_pk_bf16_f32 v233, v226, v227\n"
    "v_cvt_pk_bf16_f32 v234, v228, v229\n"
    "v_cvt_pk_bf16_f32 v235, v230, v231\n"
    "v_fma_f32 v181, v157, s52, v190\n"
    "v_add_f32 v184, v184, v180\n"
    "v_exp_f32 v181, v181\n"
    "v_fma_f32 v182, v158, s52, v190\n"
    "v_mfma_f32_32x32x16_bf16 v[80:95], a[148:151], v[164:167], v[80:95]\n"
    "ds_read_b128 a[60:63], v209 offset:8192\n"
    "ds_write_b128 v206, v[232:235] offset:0\n"
    "v_mul_f32 v224, v40, v191\n"
    "v_mul_f32 v225, v41, v191\n"
    "v_mul_f32 v226, v42, v191\n"
    "v_mul_f32 v227, v43, v191\n"
    "v_add_f32 v184, v184, v181\n"
    "v_exp_f32 v182, v182\n"
    "v_fma_f32 v183, v159, s52, v190\n"
    "v_add_f32 v184, v184, v182\n"
    "v_mfma_f32_32x32x16_bf16 v[96:111], a[152:155], v[164:167], v[96:111]\n"
    "v_exp_f32 v183, v183\n"
    "v_cvt_pk_bf16_f32 v174, v180, v181\n"
    "v_add_f32 v184, v184, v183\n"
    "v_cvt_pk_bf16_f32 v175, v182, v183\n"
    "v_cmp_nge_f32 vcc, s53, v184\n"
    "v_mul_f32 v228, v44, v191\n"
    "v_mul_f32 v229, v45, v191\n"
    "v_mul_f32 v230, v46, v191\n"
    "v_mul_f32 v231, v47, v191\n"
    "v_cvt_pk_bf16_f32 v236, v224, v225\n"
    "v_mfma_f32_32x32x16_bf16 v[112:127], a[156:159], v[164:167], v[112:127]\n"
    "s_cbranch_vccnz rare_stub_35\n"
    "back_35:\n"
    "v_add_f32 v187, v187, v184\n"
    "v_cvt_pk_bf16_f32 v237, v226, v227\n"
    "v_cvt_pk_bf16_f32 v238, v228, v229\n"
    "v_cvt_pk_bf16_f32 v239, v230, v231\n"
    "ds_write_b128 v207, v[236:239] offset:0\n"
    "v_mul_f32 v224, v48, v191\n"
    "s_waitcnt lgkmcnt(1)\n"
    "v_mfma_f32_32x32x16_bf16 v[144:159], a[96:99], a[32:35], 0\n"
    "s_waitcnt vmcnt(8)\n"
    "ds_read_b128 a[64:67], v202 offset:16384\n"
    "v_mul_f32 v225, v49, v191\n"
    "v_mul_f32 v226, v50, v191\n"
    "v_mul_f32 v227, v51, v191\n"
    "v_mul_f32 v228, v52, v191\n"
    "v_mul_f32 v229, v53, v191\n"
    "v_mul_f32 v230, v54, v191\n"
    "v_max3_f32 v248, v128, v129, v130\n"
    "v_max3_f32 v248, v248, v131, v132\n"
    "v_max3_f32 v248, v248, v133, v134\n"
    "v_max3_f32 v248, v248, v135, v136\n"
    "v_max3_f32 v248, v248, v137, v138\n"
    "v_max3_f32 v248, v248, v139, v140\n"
    "v_max3_f32 v248, v248, v141, v142\n"
    "v_max_f32 v248, v248, v143\n"
    "v_mov_b32 v249, v248\n"
    "s_nop 1\n"
    "v_permlane32_swap_b32 v249, v248\n"
    "v_max_f32 v248, v248, v249\n"
    "v_mul_f32_e64 v188, -s52, v248\n"
    "v_fma_f32 v176, v128, s52, v188\n"
    "v_exp_f32 v176, v176\n"
    "v_fma_f32 v177, v129, s52, v188\n"
    "v_exp_f32 v177, v177\n"
    "v_mfma_f32_32x32x16_bf16 v[144:159], a[100:103], a[36:39], v[144:159]\n"
    "ds_read_b128 a[68:71], v203 offset:16384\n"
    "v_mul_f32 v231, v55, v191\n"
    "v_cvt_pk_bf16_f32 v232, v224, v225\n"
    "v_cvt_pk_bf16_f32 v233, v226, v227\n"
    "v_cvt_pk_bf16_f32 v234, v228, v229\n"
    "v_cvt_pk_bf16_f32 v235, v230, v231\n"
    "v_fma_f32 v178, v130, s52, v188\n"
    "v_add_f32 v185, v176, v177\n"
    "v_cvt_pk_bf16_f32 v160, v176, v177\n"
    "v_exp_f32 v178, v178\n"
    "v_mfma_f32_32x32x16_bf16 v[144:159], a[104:107], a[40:43], v[144:159]\n"
    "ds_read_b128 a[72:75], v204 offset:16384\n"
    "v_mov_b32 v252, v187\n"
    "ds_write_b128 v208, v[232:235] offset:0\n"
    "v_mul_f32 v224, v56, v191\n"
    "v_mul_f32 v225, v57, v191\n"
    "v_mul_f32 v226, v58, v191\n"
    "v_mul_f32 v227, v59, v191\n"
    "v_fma_f32 v179, v131, s52, v188\n"
    "v_add_f32 v185, v185, v178\n"
    "v_exp_f32 v179, v179\n"
    "v_fma_f32 v180, v132, s52, v188\n"
    "v_mfma_f32_32x32x16_bf16 v[144:159], a[108:111], a[44:47], v[144:159]\n"
    "ds_read_b128 a[76:79], v205 offset:16384\n"
    "v_mul_f32 v228, v60, v191\n"
    "v_mul_f32 v229, v61, v191\n"
    "v_mul_f32 v230, v62, v191\n"
    "v_mul_f32 v231, v63, v191\n"
    "v_cvt_pk_bf16_f32 v236, v224, v225\n"
    "v_cvt_pk_bf16_f32 v237, v226, v227\n"
    "v_add_f32 v185, v185, v179\n"
    "v_cvt_pk_bf16_f32 v161, v178, v179\n"
    "v_exp_f32 v180, v180\n"
    "v_fma_f32 v181, v133, s52, v188\n"
    "v_mfma_f32_32x32x16_bf16 v[144:159], a[112:115], a[48:51], v[144:159]\n"
    "ds_read_b128 a[80:83], v206 offset:16384\n"
    "v_cvt_pk_bf16_f32 v238, v228, v229\n"
    "v_cvt_pk_bf16_f32 v239, v230, v231\n"
    "ds_write_b128 v209, v[236:239] offset:0\n"
    "v_mul_f32 v224, v64, v192\n"
    "v_mul_f32 v225, v65, v192\n"
    "v_add_f32 v185, v185, v180\n"
    "v_exp_f32 v181, v181\n"
    "v_fma_f32 v182, v134, s52, v188\n"
    "v_add_f32 v185, v185, v181\n"
    "v_mfma_f32_32x32x16_bf16 v[144:159], a[116:119], a[52:55], v[144:159]\n"
    "ds_read_b128 a[84:87], v207 offset:16384\n"
    "v_xor_b32 v213, 0x8000, v213\n"
    "v_permlane32_swap_b32 v252, v187\n"
    "v_mul_f32 v226, v66, v192\n"
    "v_mul_f32 v227, v67, v192\n"
    "v_mul_f32 v228, v68, v192\n"
    "v_mul_f32 v229, v69, v192\n"
    "v_mul_f32 v230, v70, v192\n"
    "v_cvt_pk_bf16_f32 v162, v180, v181\n"
    "v_exp_f32 v182, v182\n"
    "v_fma_f32 v183, v135, s52, v188\n"
    "v_add_f32 v185, v185, v182\n"
    "v_mfma_f32_32x32x16_bf16 v[144:159], a[120:123], a[56:59], v[144:159]\n"
    "ds_read_b128 a[88:91], v208 offset:16384\n"
    "v_mul_f32 v231, v71, v192\n"
    "v_cvt_pk_bf16_f32 v232, v224, v225\n"
    "v_cvt_pk_bf16_f32 v233, v226, v227\n"
    "v_cvt_pk_bf16_f32 v234, v228, v229\n"
    "v_cvt_pk_bf16_f32 v235, v230, v231\n"
    "ds_write_b128 v202, v[232:235] offset:8192\n"
    "v_exp_f32 v183, v183\n"
    "v_fma_f32 v176, v136, s52, v188\n"
    "v_add_f32 v185, v185, v183\n"
    "v_cvt_pk_bf16_f32 v163, v182, v183\n"
    "v_mfma_f32_32x32x16_bf16 v[144:159], a[124:127], a[60:63], v[144:159]\n"
    "ds_read_b128 a[92:95], v209 offset:16384\n"
    "v_add_f32 v187, v187, v252\n"
    "v_mul_f32 v224, v72, v192\n"
    "v_mul_f32 v225, v73, v192\n"
    "v_mul_f32 v226, v74, v192\n"
    "v_mul_f32 v227, v75, v192\n"
    "v_mul_f32 v228, v76, v192\n"
    "v_exp_f32 v176, v176\n"
    "v_fma_f32 v177, v137, s52, v188\n"
    "v_add_f32 v185, v185, v176\n"
    "v_exp_f32 v177, v177\n"
    "v_mfma_f32_32x32x16_bf16 a[160:175], a[128:131], v[168:171], a[160:175]\n"
    "s_barrier\n"
    "v_fma_f32 v178, v138, s52, v188\n"
    "v_mfma_f32_32x32x16_bf16 a[176:191], a[132:135], v[168:171], a[176:191]\n"
    "ds_read_b64_tr_b16 a[128:129], v210 offset:0\n"
    "v_mul_f32 v229, v77, v192\n"
    "v_mul_f32 v230, v78, v192\n"
    "v_mul_f32 v231, v79, v192\n"
    "v_cvt_pk_bf16_f32 v236, v224, v225\n"
    "v_cvt_pk_bf16_f32 v237, v226, v227\n"
    "v_cvt_pk_bf16_f32 v238, v228, v229\n"
    "v_cvt_pk_bf16_f32 v239, v230, v231\n"
    "ds_write_b128 v203, v[236:239] offset:8192\n"
    "v_mul_f32 v224, v80, v192\n"
    "v_mul_f32 v225, v81, v192\n"
    "v_add_f32 v185, v185, v177\n"
    "v_cvt_pk_bf16_f32 v164, v176, v177\n"
    "v_exp_f32 v178, v178\n"
    "v_mfma_f32_32x32x16_bf16 a[192:207], a[136:139], v[168:171], a[192:207]\n"
    "s_add_u32 m0, s56, 16384\n"
    "ds_read_b64_tr_b16 a[130:131], v210 offset:2048\n"
    "buffer_load_dwordx4 v220, s[40:43], s54 offen nt lds\n"
    "v_mul_f32 v226, v82, v192\n"
    "v_mul_f32 v227, v83, v192\n"
    "v_mul_f32 v228, v84, v192\n"
    "v_mul_f32 v229, v85, v192\n"
    "v_mul_f32 v230, v86, v192\n"
    "v_mul_f32 v231, v87, v192\n"
    "v_fma_f32 v179, v139, s52, v188\n"
    "v_add_f32 v185, v185, v178\n"
    "v_exp_f32 v179, v179\n"
    "v_mfma_f32_32x32x16_bf16 a[208:223], a[140:143], v[168:171], a[208:223]\n"
    "ds_read_b64_tr_b16 a[132:133], v211 offset:0\n"
    "v_cvt_pk_bf16_f32 v232, v224, v225\n"
    "v_cvt_pk_bf16_f32 v233, v226, v227\n"
    "v_cvt_pk_bf16_f32 v234, v228, v229\n"
    "v_cvt_pk_bf16_f32 v235, v230, v231\n"
    "ds_write_b128 v204, v[232:235] offset:8192\n"
    "v_fma_f32 v180, v140, s52, v188\n"
    "v_add_f32 v185, v185, v179\n"
    "v_cvt_pk_bf16_f32 v165, v178, v179\n"
    "v_exp_f32 v180, v180\n"
    "v_mfma_f32_32x32x16_bf16 a[160:175], a[144:147], v[172:175], a[160:175]\n"
    "ds_read_b64_tr_b16 a[134:135], v211 offset:2048\n"
    "buffer_load_dwordx4 v220, s[40:43], s54 offen offset:1024 nt lds\n"
    "v_rcp_f32 v193, v187\n"
    "v_mul_f32 v224, v88, v192\n"
    "v_mul_f32 v225, v89, v192\n"
    "v_mul_f32 v226, v90, v192\n"
    "v_mul_f32 v227, v91, v192\n"
    "v_mul_f32 v228, v92, v192\n"
    "v_fma_f32 v181, v141, s52, v188\n"
    "v_add_f32 v185, v185, v180\n"
    "v_mfma_f32_32x32x16_bf16 a[176:191], a[148:151], v[172:175], a[176:191]\n"
    "s_add_u32 m0, s56, 24576\n"
    "ds_read_b64_tr_b16 a[136:137], v212 offset:0\n"
    "v_mul_f32 v229, v93, v192\n"
    "v_mul_f32 v230, v94, v192\n"
    "v_mul_f32 v231, v95, v192\n"
    "v_cvt_pk_bf16_f32 v236, v224, v225\n"
    "v_cvt_pk_bf16_f32 v237, v226, v227\n"
    "v_cvt_pk_bf16_f32 v238, v228, v229\n"
    "v_exp_f32 v181, v181\n"
    "v_fma_f32 v182, v142, s52, v188\n"
    "v_add_f32 v185, v185, v181\n"
    "v_exp_f32 v182, v182\n"
    "v_mfma_f32_32x32x16_bf16 a[192:207], a[152:155], v[172:175], a[192:207]\n"
    "ds_read_b64_tr_b16 a[138:139], v212 offset:2048\n"
    "buffer_load_dwordx4 v218, s[36:39], s63 offen nt lds\n"
    "v_xor_b32 v194, 0x8000, v194\n"
    "v_cvt_pk_bf16_f32 v239, v230, v231\n"
    "ds_write_b128 v205, v[236:239] offset:8192\n"
    "v_mul_f32 v224, v96, v192\n"
    "v_mul_f32 v225, v97, v192\n"
    "v_mul_f32 v226, v98, v192\n"
    "v_fma_f32 v183, v143, s52, v188\n"
    "v_add_f32 v185, v185, v182\n"
    "v_mfma_f32_32x32x16_bf16 a[208:223], a[156:159], v[172:175], a[208:223]\n"
    "s_waitcnt lgkmcnt(3)\n"
    "ds_read_b64_tr_b16 a[140:141], v213 offset:0\n"
    "v_xor_b32 v195, 0x8000, v195\n"
    "v_exp_f32 v183, v183\n"
    "v_cvt_pk_bf16_f32 v166, v180, v181\n"
    "v_add_f32 v185, v185, v183\n"
    "v_cvt_pk_bf16_f32 v167, v182, v183\n"
    "v_mul_f32 v227, v99, v192\n"
    "v_mul_f32 v228, v100, v192\n"
    "v_mul_f32 v229, v101, v192\n"
    "v_mul_f32 v230, v102, v192\n"
    "v_mul_f32 v231, v103, v192\n"
    "v_mfma_f32_32x32x16_bf16 v[128:143], a[96:99], a[64:67], 0\n"
    "ds_read_b64_tr_b16 a[142:143], v213 offset:2048\n"
    "buffer_load_dwordx4 v219, s[36:39], s63 offen offset:1024 nt lds\n"
    "v_xor_b32 v196, 0x8000, v196\n"
    "v_cvt_pk_bf16_f32 v232, v224, v225\n"
    "v_cvt_pk_bf16_f32 v233, v226, v227\n"
    "v_cvt_pk_bf16_f32 v234, v228, v229\n"
    "v_cvt_pk_bf16_f32 v235, v230, v231\n"
    "ds_write_b128 v206, v[232:235] offset:8192\n"
    "v_max3_f32 v248, v144, v145, v146\n"
    "v_max3_f32 v248, v248, v147, v148\n"
    "v_max3_f32 v248, v248, v149, v150\n"
    "v_max3_f32 v248, v248, v151, v152\n"
    "v_max3_f32 v248, v248, v153, v154\n"
    "v_max3_f32 v248, v248, v155, v156\n"
    "v_max3_f32 v248, v248, v157, v158\n"
    "v_max_f32 v248, v248, v159\n"
    "v_mov_b32 v249, v248\n"
    "v_mfma_f32_32x32x16_bf16 v[128:143], a[100:103], a[68:71], v[128:143]\n"
    "ds_read_b64_tr_b16 a[144:145], v210 offset:4096\n"
    "v_xor_b32 v197, 0x8000, v197\n"
    "v_mul_f32 v224, v104, v192\n"
    "v_mul_f32 v225, v105, v192\n"
    "v_mul_f32 v226, v106, v192\n"
    "v_mul_f32 v227, v107, v192\n"
    "v_mul_f32 v228, v108, v192\n"
    "v_mul_f32 v229, v109, v192\n"
    "v_permlane32_swap_b32 v249, v248\n"
    "v_max_f32 v248, v248, v249\n"
    "v_mul_f32_e64 v189, -s52, v248\n"
    "v_fma_f32 v176, v144, s52, v189\n"
    "v_exp_f32 v176, v176\n"
    "v_mfma_f32_32x32x16_bf16 v[128:143], a[104:107], a[72:75], v[128:143]\n"
    "ds_read_b64_tr_b16 a[146:147], v210 offset:6144\n"
    "v_xor_b32 v198, 0x8000, v198\n"
    "v_mul_f32 v230, v110, v192\n"
    "v_mul_f32 v231, v111, v192\n"
    "v_cvt_pk_bf16_f32 v236, v224, v225\n"
    "v_cvt_pk_bf16_f32 v237, v226, v227\n"
    "v_cvt_pk_bf16_f32 v238, v228, v229\n"
    "v_fma_f32 v177, v145, s52, v189\n"
    "v_exp_f32 v177, v177\n"
    "v_fma_f32 v178, v146, s52, v189\n"
    "v_mfma_f32_32x32x16_bf16 v[128:143], a[108:111], a[76:79], v[128:143]\n"
    "ds_read_b64_tr_b16 a[148:149], v211 offset:4096\n"
    "v_xor_b32 v199, 0x8000, v199\n"
    "v_cvt_pk_bf16_f32 v239, v230, v231\n"
    "ds_write_b128 v207, v[236:239] offset:8192\n"
    "v_mul_f32 v224, v112, v192\n"
    "v_mul_f32 v225, v113, v192\n"
    "v_mul_f32 v226, v114, v192\n"
    "v_add_f32 v186, v176, v177\n"
    "v_cvt_pk_bf16_f32 v168, v176, v177\n"
    "v_exp_f32 v178, v178\n"
    "v_fma_f32 v179, v147, s52, v189\n"
    "v_mfma_f32_32x32x16_bf16 v[128:143], a[112:115], a[80:83], v[128:143]\n"
    "ds_read_b64_tr_b16 a[150:151], v211 offset:6144\n"
    "v_xor_b32 v200, 0x8000, v200\n"
    "v_mov_b32 v222, v221\n"
    "v_mul_f32 v227, v115, v192\n"
    "v_mul_f32 v228, v116, v192\n"
    "v_mul_f32 v229, v117, v192\n"
    "v_mul_f32 v230, v118, v192\n"
    "v_mul_f32 v231, v119, v192\n"
    "v_cvt_pk_bf16_f32 v232, v224, v225\n"
    "v_add_f32 v186, v186, v178\n"
    "v_exp_f32 v179, v179\n"
    "v_fma_f32 v180, v148, s52, v189\n"
    "v_add_f32 v186, v186, v179\n"
    "v_mfma_f32_32x32x16_bf16 v[128:143], a[116:119], a[84:87], v[128:143]\n"
    "ds_read_b64_tr_b16 a[152:153], v212 offset:4096\n"
    "v_xor_b32 v201, 0x8000, v201\n"
    "v_cvt_pk_bf16_f32 v233, v226, v227\n"
    "v_cvt_pk_bf16_f32 v234, v228, v229\n"
    "v_cvt_pk_bf16_f32 v235, v230, v231\n"
    "ds_write_b128 v208, v[232:235] offset:8192\n"
    "v_mul_f32 v224, v120, v192\n"
    "v_cvt_pk_bf16_f32 v169, v178, v179\n"
    "v_exp_f32 v180, v180\n"
    "v_fma_f32 v181, v149, s52, v189\n"
    "v_add_f32 v186, v186, v180\n"
    "v_mfma_f32_32x32x16_bf16 v[128:143], a[120:123], a[88:91], v[128:143]\n"
    "ds_read_b64_tr_b16 a[154:155], v212 offset:6144\n"
    "v_mul_f32 v225, v121, v192\n"
    "v_mul_f32 v226, v122, v192\n"
    "v_mul_f32 v227, v123, v192\n"
    "v_mul_f32 v228, v124, v192\n"
    "v_mul_f32 v229, v125, v192\n"
    "v_exp_f32 v181, v181\n"
    "v_fma_f32 v182, v150, s52, v189\n"
    "v_add_f32 v186, v186, v181\n"
    "v_cvt_pk_bf16_f32 v170, v180, v181\n"
    "v_mfma_f32_32x32x16_bf16 v[128:143], a[124:127], a[92:95], v[128:143]\n"
    "ds_read_b64_tr_b16 a[156:157], v213 offset:4096\n"
    "v_mul_f32 v230, v126, v192\n"
    "v_mul_f32 v231, v127, v192\n"
    "v_cvt_pk_bf16_f32 v236, v224, v225\n"
    "v_cvt_pk_bf16_f32 v237, v226, v227\n"
    "v_cvt_pk_bf16_f32 v238, v228, v229\n"
    "v_cvt_pk_bf16_f32 v239, v230, v231\n"
    "v_exp_f32 v182, v182\n"
    "v_fma_f32 v183, v151, s52, v189\n"
    "v_add_f32 v186, v186, v182\n"
    "v_exp_f32 v183, v183\n"
    "v_mfma_f32_32x32x16_bf16 v[0:15], a[128:131], v[160:163], 0\n"
    "ds_read_b64_tr_b16 a[158:159], v213 offset:6144\n"
    "ds_read_b128 a[96:99], v194 offset:8192\n"
    "ds_write_b128 v209, v[236:239] offset:8192\n"
    "v_accvgpr_read_b32 v224, a160\n"
    "v_accvgpr_read_b32 v225, a161\n"
    "v_accvgpr_read_b32 v226, a162\n"
    "v_accvgpr_read_b32 v227, a163\n"
    "v_fma_f32 v176, v152, s52, v189\n"
    "v_add_f32 v186, v186, v183\n"
    "v_cvt_pk_bf16_f32 v171, v182, v183\n"
    "v_mfma_f32_32x32x16_bf16 v[16:31], a[132:135], v[160:163], 0\n"
    "s_waitcnt lgkmcnt(4)\n"
    "ds_read_b128 a[100:103], v195 offset:8192\n"
    "v_accvgpr_read_b32 v228, a164\n"
    "v_accvgpr_read_b32 v229, a165\n"
    "v_accvgpr_read_b32 v230, a166\n"
    "v_accvgpr_read_b32 v231, a167\n"
    "v_mul_f32 v224, v224, v193\n"
    "v_exp_f32 v176, v176\n"
    "v_fma_f32 v177, v153, s52, v189\n"
    "v_add_f32 v186, v186, v176\n"
    "v_exp_f32 v177, v177\n"
    "v_mfma_f32_32x32x16_bf16 v[32:47], a[136:139], v[160:163], 0\n"
    "ds_read_b128 a[104:107], v196 offset:8192\n"
    "v_mul_f32 v225, v225, v193\n"
    "v_mul_f32 v226, v226, v193\n"
    "v_mul_f32 v227, v227, v193\n"
    "v_mul_f32 v228, v228, v193\n"
    "v_mul_f32 v229, v229, v193\n"
    "v_fma_f32 v178, v154, s52, v189\n"
    "v_add_f32 v186, v186, v177\n"
    "v_cvt_pk_bf16_f32 v172, v176, v177\n"
    "v_exp_f32 v178, v178\n"
    "v_mfma_f32_32x32x16_bf16 v[48:63], a[140:143], v[160:163], 0\n"
    "ds_read_b128 a[108:111], v197 offset:8192\n"
    "v_mul_f32 v230, v230, v193\n"
    "v_mul_f32 v231, v231, v193\n"
    "v_cvt_pk_bf16_f32 v232, v224, v225\n"
    "v_cvt_pk_bf16_f32 v233, v226, v227\n"
    "v_cvt_pk_bf16_f32 v234, v228, v229\n"
    "v_cvt_pk_bf16_f32 v235, v230, v231\n"
    "v_fma_f32 v179, v155, s52, v189\n"
    "v_add_f32 v186, v186, v178\n"
    "v_exp_f32 v179, v179\n"
    "v_mfma_f32_32x32x16_bf16 v[0:15], a[144:147], v[164:167], v[0:15]\n"
    "ds_read_b128 a[112:115], v198 offset:8192\n"
    "ds_write_b128 v202, v[232:235] offset:16384\n"
    "v_accvgpr_read_b32 v224, a168\n"
    "v_accvgpr_read_b32 v225, a169\n"
    "v_accvgpr_read_b32 v226, a170\n"
    "v_accvgpr_read_b32 v227, a171\n"
    "v_fma_f32 v180, v156, s52, v189\n"
    "v_add_f32 v186, v186, v179\n"
    "v_cvt_pk_bf16_f32 v173, v178, v179\n"
    "v_exp_f32 v180, v180\n"
    "v_mfma_f32_32x32x16_bf16 v[16:31], a[148:151], v[164:167], v[16:31]\n"
    "ds_read_b128 a[116:119], v199 offset:8192\n"
    "v_accvgpr_read_b32 v228, a172\n"
    "v_accvgpr_read_b32 v229, a173\n"
    "v_accvgpr_read_b32 v230, a174\n"
    "v_accvgpr_read_b32 v231, a175\n"
    "v_mul_f32 v224, v224, v193\n"
    "v_fma_f32 v181, v157, s52, v189\n"
    "v_add_f32 v186, v186, v180\n"
    "v_exp_f32 v181, v181\n"
    "v_fma_f32 v182, v158, s52, v189\n"
    "v_mfma_f32_32x32x16_bf16 v[32:47], a[152:155], v[164:167], v[32:47]\n"
    "s_waitcnt lgkmcnt(3)\n"
    "ds_read_b128 a[120:123], v200 offset:8192\n"
    "v_mul_f32 v225, v225, v193\n"
    "v_mul_f32 v226, v226, v193\n"
    "v_mul_f32 v227, v227, v193\n"
    "v_mul_f32 v228, v228, v193\n"
    "v_mul_f32 v229, v229, v193\n"
    "v_mul_f32 v230, v230, v193\n"
    "v_add_f32 v186, v186, v181\n"
    "v_exp_f32 v182, v182\n"
    "v_fma_f32 v183, v159, s52, v189\n"
    "v_add_f32 v186, v186, v182\n"
    "v_mfma_f32_32x32x16_bf16 v[48:63], a[156:159], v[164:167], v[48:63]\n"
    "ds_read_b128 a[124:127], v201 offset:8192\n"
    "v_exp_f32 v183, v183\n"
    "v_cvt_pk_bf16_f32 v174, v180, v181\n"
    "v_add_f32 v186, v186, v183\n"
    "v_cvt_pk_bf16_f32 v175, v182, v183\n"
    "v_mul_f32 v231, v231, v193\n"
    "v_cvt_pk_bf16_f32 v236, v224, v225\n"
    "v_cvt_pk_bf16_f32 v237, v226, v227\n"
    "v_cvt_pk_bf16_f32 v238, v228, v229\n"
    "v_cvt_pk_bf16_f32 v239, v230, v231\n"
    "v_mfma_f32_32x32x16_bf16 v[144:159], a[96:99], a[0:3], 0\n"
    "ds_write_b128 v203, v[236:239] offset:16384\n"
    "v_accvgpr_read_b32 v224, a176\n"
    "v_accvgpr_read_b32 v225, a177\n"
    "v_accvgpr_read_b32 v226, a178\n"
    "v_accvgpr_read_b32 v227, a179\n"
    "v_max3_f32 v248, v128, v129, v130\n"
    "v_max3_f32 v248, v248, v131, v132\n"
    "v_max3_f32 v248, v248, v133, v134\n"
    "v_max3_f32 v248, v248, v135, v136\n"
    "v_max3_f32 v248, v248, v137, v138\n"
    "v_max3_f32 v248, v248, v139, v140\n"
    "v_max3_f32 v248, v248, v141, v142\n"
    "v_max_f32 v248, v248, v143\n"
    "v_mov_b32 v249, v248\n"
    "s_nop 1\n"
    "v_permlane32_swap_b32 v249, v248\n"
    "v_max_f32 v248, v248, v249\n"
    "v_mul_f32_e64 v190, -s52, v248\n"
    "v_fma_f32 v176, v128, s52, v190\n"
    "v_mfma_f32_32x32x16_bf16 v[144:159], a[100:103], a[4:7], v[144:159]\n"
    "s_mov_b32 s54, s63\n"
    "v_accvgpr_read_b32 v228, a180\n"
    "v_accvgpr_read_b32 v229, a181\n"
    "v_accvgpr_read_b32 v230, a182\n"
    "v_accvgpr_read_b32 v231, a183\n"
    "v_mul_f32 v224, v224, v193\n"
    "v_exp_f32 v176, v176\n"
    "v_fma_f32 v177, v129, s52, v190\n"
    "v_exp_f32 v177, v177\n"
    "v_fma_f32 v178, v130, s52, v190\n"
    "v_mfma_f32_32x32x16_bf16 v[144:159], a[104:107], a[8:11], v[144:159]\n"
    "s_add_u32 s63, s63, 8192\n"
    "v_mul_f32 v225, v225, v193\n"
    "v_mul_f32 v226, v226, v193\n"
    "v_mul_f32 v227, v227, v193\n"
    "v_mul_f32 v228, v228, v193\n"
    "v_mul_f32 v229, v229, v193\n"
    "v_mul_f32 v230, v230, v193\n"
    "v_add_f32 v187, v176, v177\n"
    "v_cvt_pk_bf16_f32 v160, v176, v177\n"
    "v_exp_f32 v178, v178\n"
    "v_fma_f32 v179, v131, s52, v190\n"
    "v_add_f32 v187, v187, v178\n"
    "v_mfma_f32_32x32x16_bf16 v[144:159], a[108:111], a[12:15], v[144:159]\n"
    "s_waitcnt lgkmcnt(0)\n"
    "s_xor_b32 s56, s56, 32768\n"
    "v_mul_f32 v231, v231, v193\n"
    "v_cvt_pk_bf16_f32 v232, v224, v225\n"
    "v_cvt_pk_bf16_f32 v233, v226, v227\n"
    "v_cvt_pk_bf16_f32 v234, v228, v229\n"
    "v_cvt_pk_bf16_f32 v235, v230, v231\n"
    "v_exp_f32 v179, v179\n"
    "v_fma_f32 v180, v132, s52, v190\n"
    "v_add_f32 v187, v187, v179\n"
    "v_cvt_pk_bf16_f32 v161, v178, v179\n"
    "v_exp_f32 v180, v180\n"
    "v_mfma_f32_32x32x16_bf16 v[144:159], a[112:115], a[16:19], v[144:159]\n"
    "ds_write_b128 v204, v[232:235] offset:16384\n"
    "v_accvgpr_read_b32 v224, a184\n"
    "v_accvgpr_read_b32 v225, a185\n"
    "v_accvgpr_read_b32 v226, a186\n"
    "v_accvgpr_read_b32 v227, a187\n"
    "v_fma_f32 v181, v133, s52, v190\n"
    "v_add_f32 v187, v187, v180\n"
    "v_exp_f32 v181, v181\n"
    "v_fma_f32 v182, v134, s52, v190\n"
    "v_mfma_f32_32x32x16_bf16 v[144:159], a[116:119], a[20:23], v[144:159]\n"
    "v_accvgpr_read_b32 v228, a188\n"
    "v_accvgpr_read_b32 v229, a189\n"
    "v_accvgpr_read_b32 v230, a190\n"
    "v_accvgpr_read_b32 v231, a191\n"
    "v_mul_f32 v224, v224, v193\n"
    "v_mul_f32 v225, v225, v193\n"
    "v_add_f32 v187, v187, v181\n"
    "v_cvt_pk_bf16_f32 v162, v180, v181\n"
    "v_exp_f32 v182, v182\n"
    "v_fma_f32 v183, v135, s52, v190\n"
    "v_add_f32 v187, v187, v182\n"
    "v_mfma_f32_32x32x16_bf16 v[144:159], a[120:123], a[24:27], v[144:159]\n"
    "s_mov_b32 s55, 0\n"
    "ds_read_b128 v[240:243], v222 offset:0\n"
    "v_mul_f32 v226, v226, v193\n"
    "v_mul_f32 v227, v227, v193\n"
    "v_mul_f32 v228, v228, v193\n"
    "v_mul_f32 v229, v229, v193\n"
    "v_mul_f32 v230, v230, v193\n"
    "v_exp_f32 v183, v183\n"
    "v_fma_f32 v176, v136, s52, v190\n"
    "v_add_f32 v187, v187, v183\n"
    "v_cvt_pk_bf16_f32 v163, v182, v183\n"
    "v_mfma_f32_32x32x16_bf16 v[144:159], a[124:127], a[28:31], v[144:159]\n"
    "v_mul_f32 v231, v231, v193\n"
    "v_cvt_pk_bf16_f32 v236, v224, v225\n"
    "v_cvt_pk_bf16_f32 v237, v226, v227\n"
    "v_cvt_pk_bf16_f32 v238, v228, v229\n"
    "v_cvt_pk_bf16_f32 v239, v230, v231\n"
    "v_exp_f32 v176, v176\n"
    "v_fma_f32 v177, v137, s52, v190\n"
    "v_add_f32 v187, v187, v176\n"
    "v_exp_f32 v177, v177\n"
    "v_mfma_f32_32x32x16_bf16 v[64:79], a[128:131], v[168:171], 0\n"
    "s_waitcnt lgkmcnt(0)\n"
    "buffer_store_dwordx4 v[240:243], v214, s[48:51], s55 offen nt\n"
    "ds_read_b128 v[244:247], v222 offset:1024\n"
    "ds_write_b128 v205, v[236:239] offset:16384\n"
    "v_accvgpr_read_b32 v224, a192\n"
    "v_accvgpr_read_b32 v225, a193\n"
    "v_accvgpr_read_b32 v226, a194\n"
    "v_accvgpr_read_b32 v227, a195\n"
    "v_accvgpr_read_b32 v228, a196\n"
    "v_fma_f32 v178, v138, s52, v190\n"
    "v_mfma_f32_32x32x16_bf16 v[80:95], a[132:135], v[168:171], 0\n"
    "v_accvgpr_read_b32 v229, a197\n"
    "v_accvgpr_read_b32 v230, a198\n"
    "v_accvgpr_read_b32 v231, a199\n"
    "v_mul_f32 v224, v224, v193\n"
    "v_mul_f32 v225, v225, v193\n"
    "v_add_f32 v187, v187, v177\n"
    "v_cvt_pk_bf16_f32 v164, v176, v177\n"
    "v_exp_f32 v178, v178\n"
    "v_fma_f32 v179, v139, s52, v190\n"
    "v_add_f32 v187, v187, v178\n"
    "v_mfma_f32_32x32x16_bf16 v[96:111], a[136:139], v[168:171], 0\n"
    "s_waitcnt lgkmcnt(1)\n"
    "buffer_store_dwordx4 v[244:247], v215, s[48:51], s55 offen offset:1024 nt\n"
    "ds_read_b128 v[240:243], v222 offset:2048\n"
    "v_mul_f32 v226, v226, v193\n"
    "v_mul_f32 v227, v227, v193\n"
    "v_mul_f32 v228, v228, v193\n"
    "v_mul_f32 v229, v229, v193\n"
    "v_mul_f32 v230, v230, v193\n"
    "v_exp_f32 v179, v179\n"
    "v_mfma_f32_32x32x16_bf16 v[112:127], a[140:143], v[168:171], 0\n"
    "v_mul_f32 v231, v231, v193\n"
    "v_cvt_pk_bf16_f32 v232, v224, v225\n"
    "v_cvt_pk_bf16_f32 v233, v226, v227\n"
    "v_cvt_pk_bf16_f32 v234, v228, v229\n"
    "v_cvt_pk_bf16_f32 v235, v230, v231\n"
    "v_fma_f32 v180, v140, s52, v190\n"
    "v_add_f32 v187, v187, v179\n"
    "v_cvt_pk_bf16_f32 v165, v178, v179\n"
    "v_exp_f32 v180, v180\n"
    "v_mfma_f32_32x32x16_bf16 v[64:79], a[144:147], v[172:175], v[64:79]\n"
    "s_waitcnt lgkmcnt(0)\n"
    "buffer_store_dwordx4 v[240:243], v216, s[48:51], s55 offen offset:2048 nt\n"
    "ds_read_b128 v[244:247], v222 offset:3072\n"
    "ds_write_b128 v206, v[232:235] offset:16384\n"
    "v_accvgpr_read_b32 v224, a200\n"
    "v_accvgpr_read_b32 v225, a201\n"
    "v_accvgpr_read_b32 v226, a202\n"
    "v_accvgpr_read_b32 v227, a203\n"
    "v_accvgpr_read_b32 v228, a204\n"
    "v_fma_f32 v181, v141, s52, v190\n"
    "v_mfma_f32_32x32x16_bf16 v[80:95], a[148:151], v[172:175], v[80:95]\n"
    "v_accvgpr_read_b32 v229, a205\n"
    "v_accvgpr_read_b32 v230, a206\n"
    "v_accvgpr_read_b32 v231, a207\n"
    "v_mul_f32 v224, v224, v193\n"
    "v_mul_f32 v225, v225, v193\n"
    "v_add_f32 v187, v187, v180\n"
    "v_exp_f32 v181, v181\n"
    "v_fma_f32 v182, v142, s52, v190\n"
    "v_add_f32 v187, v187, v181\n"
    "v_exp_f32 v182, v182\n"
    "v_mfma_f32_32x32x16_bf16 v[96:111], a[152:155], v[172:175], v[96:111]\n"
    "s_waitcnt lgkmcnt(1)\n"
    "buffer_store_dwordx4 v[244:247], v217, s[48:51], s55 offen offset:3072 nt\n"
    "v_mul_f32 v226, v226, v193\n"
    "v_mul_f32 v227, v227, v193\n"
    "v_mul_f32 v228, v228, v193\n"
    "v_mul_f32 v229, v229, v193\n"
    "v_mul_f32 v230, v230, v193\n"
    "v_fma_f32 v183, v143, s52, v190\n"
    "v_mfma_f32_32x32x16_bf16 v[112:127], a[156:159], v[172:175], v[112:127]\n"
    "v_add_f32 v187, v187, v182\n"
    "v_exp_f32 v183, v183\n"
    "v_cvt_pk_bf16_f32 v166, v180, v181\n"
    "v_add_f32 v187, v187, v183\n"
    "v_cvt_pk_bf16_f32 v167, v182, v183\n"
    "v_mul_f32 v231, v231, v193\n"
    "v_cvt_pk_bf16_f32 v236, v224, v225\n"
    "v_cvt_pk_bf16_f32 v237, v226, v227\n"
    "v_cvt_pk_bf16_f32 v238, v228, v229\n"
    "v_cvt_pk_bf16_f32 v239, v230, v231\n"
    "ds_write_b128 v207, v[236:239] offset:16384\n"
    "v_mfma_f32_32x32x16_bf16 v[128:143], a[96:99], a[32:35], 0\n"
    "v_accvgpr_read_b32 v224, a208\n"
    "v_accvgpr_read_b32 v225, a209\n"
    "v_accvgpr_read_b32 v226, a210\n"
    "v_accvgpr_read_b32 v227, a211\n"
    "v_accvgpr_read_b32 v228, a212\n"
    "v_fma_f32 v176, v144, s52, v188\n"
    "v_exp_f32 v176, v176\n"
    "v_fma_f32 v177, v145, s52, v188\n"
    "v_exp_f32 v177, v177\n"
    "v_mfma_f32_32x32x16_bf16 v[128:143], a[100:103], a[36:39], v[128:143]\n"
    "v_accvgpr_read_b32 v229, a213\n"
    "v_accvgpr_read_b32 v230, a214\n"
    "v_accvgpr_read_b32 v231, a215\n"
    "v_mul_f32 v224, v224, v193\n"
    "v_mul_f32 v225, v225, v193\n"
    "v_fma_f32 v178, v146, s52, v188\n"
    "v_add_f32 v184, v176, v177\n"
    "v_cvt_pk_bf16_f32 v168, v176, v177\n"
    "v_exp_f32 v178, v178\n"
    "v_mfma_f32_32x32x16_bf16 v[128:143], a[104:107], a[40:43], v[128:143]\n"
    "v_mul_f32 v226, v226, v193\n"
    "v_mul_f32 v227, v227, v193\n"
    "v_mul_f32 v228, v228, v193\n"
    "v_mul_f32 v229, v229, v193\n"
    "v_mul_f32 v230, v230, v193\n"
    "v_mul_f32 v231, v231, v193\n"
    "v_fma_f32 v179, v147, s52, v188\n"
    "v_add_f32 v184, v184, v178\n"
    "v_exp_f32 v179, v179\n"
    "v_fma_f32 v180, v148, s52, v188\n"
    "v_add_f32 v184, v184, v179\n"
    "v_mfma_f32_32x32x16_bf16 v[128:143], a[108:111], a[44:47], v[128:143]\n"
    "v_cvt_pk_bf16_f32 v232, v224, v225\n"
    "v_cvt_pk_bf16_f32 v233, v226, v227\n"
    "v_cvt_pk_bf16_f32 v234, v228, v229\n"
    "v_cvt_pk_bf16_f32 v235, v230, v231\n"
    "ds_write_b128 v208, v[232:235] offset:16384\n"
    "v_cvt_pk_bf16_f32 v169, v178, v179\n"
    "v_exp_f32 v180, v180\n"
    "v_fma_f32 v181, v149, s52, v188\n"
    "v_add_f32 v184, v184, v180\n"
    "v_exp_f32 v181, v181\n"
    "v_mfma_f32_32x32x16_bf16 v[128:143], a[112:115], a[48:51], v[128:143]\n"
    "v_accvgpr_read_b32 v224, a216\n"
    "v_accvgpr_read_b32 v225, a217\n"
    "v_accvgpr_read_b32 v226, a218\n"
    "v_accvgpr_read_b32 v227, a219\n"
    "v_accvgpr_read_b32 v228, a220\n"
    "v_fma_f32 v182, v150, s52, v188\n"
    "v_add_f32 v184, v184, v181\n"
    "v_cvt_pk_bf16_f32 v170, v180, v181\n"
    "v_exp_f32 v182, v182\n"
    "v_mfma_f32_32x32x16_bf16 v[128:143], a[116:119], a[52:55], v[128:143]\n"
    "v_accvgpr_read_b32 v229, a221\n"
    "v_accvgpr_read_b32 v230, a222\n"
    "v_accvgpr_read_b32 v231, a223\n"
    "v_mul_f32 v224, v224, v193\n"
    "v_mul_f32 v225, v225, v193\n"
    "v_fma_f32 v183, v151, s52, v188\n"
    "v_add_f32 v184, v184, v182\n"
    "v_exp_f32 v183, v183\n"
    "v_fma_f32 v176, v152, s52, v188\n"
    "v_add_f32 v184, v184, v183\n"
    "v_mfma_f32_32x32x16_bf16 v[128:143], a[120:123], a[56:59], v[128:143]\n"
    "v_mul_f32 v226, v226, v193\n"
    "v_mul_f32 v227, v227, v193\n"
    "v_mul_f32 v228, v228, v193\n"
    "v_mul_f32 v229, v229, v193\n"
    "v_mul_f32 v230, v230, v193\n"
    "v_mul_f32 v231, v231, v193\n"
    "v_cvt_pk_bf16_f32 v171, v182, v183\n"
    "v_exp_f32 v176, v176\n"
    "v_fma_f32 v177, v153, s52, v188\n"
    "v_add_f32 v184, v184, v176\n"
    "v_exp_f32 v177, v177\n"
    "v_mfma_f32_32x32x16_bf16 v[128:143], a[124:127], a[60:63], v[128:143]\n"
    "v_cvt_pk_bf16_f32 v236, v224, v225\n"
    "v_cvt_pk_bf16_f32 v237, v226, v227\n"
    "v_cvt_pk_bf16_f32 v238, v228, v229\n"
    "v_cvt_pk_bf16_f32 v239, v230, v231\n"
    "ds_write_b128 v209, v[236:239] offset:16384\n"
    "v_fma_f32 v178, v154, s52, v188\n"
    "v_add_f32 v184, v184, v177\n"
    "v_cvt_pk_bf16_f32 v172, v176, v177\n"
    "v_exp_f32 v178, v178\n"
    "v_mfma_f32_32x32x16_bf16 a[160:175], a[128:131], v[160:163], 0\n"
    "s_waitcnt vmcnt(12)\n"
    "s_barrier\n"
    "v_fma_f32 v179, v155, s52, v188\n"
    "v_mfma_f32_32x32x16_bf16 a[176:191], a[132:135], v[160:163], 0\n"
    "ds_read_b64_tr_b16 a[128:129], v210 offset:16384\n"
    "v_add_f32 v184, v184, v178\n"
    "v_exp_f32 v179, v179\n"
    "v_fma_f32 v180, v156, s52, v188\n"
    "v_add_f32 v184, v184, v179\n"
    "v_mfma_f32_32x32x16_bf16 a[192:207], a[136:139], v[160:163], 0\n"
    "s_add_u32 m0, s56, 0\n"
    "ds_read_b64_tr_b16 a[130:131], v210 offset:18432\n"
    "buffer_load_dwordx4 v220, s[40:43], s54 offen nt lds\n"
    "v_cvt_pk_bf16_f32 v173, v178, v179\n"
    "v_exp_f32 v180, v180\n"
    "v_mfma_f32_32x32x16_bf16 a[208:223], a[140:143], v[160:163], 0\n"
    "ds_read_b64_tr_b16 a[132:133], v211 offset:16384\n"
    "v_fma_f32 v181, v157, s52, v188\n"
    "v_add_f32 v184, v184, v180\n"
    "v_exp_f32 v181, v181\n"
    "v_fma_f32 v182, v158, s52, v188\n"
    "v_mfma_f32_32x32x16_bf16 a[160:175], a[144:147], v[164:167], a[160:175]\n"
    "ds_read_b64_tr_b16 a[134:135], v211 offset:18432\n"
    "buffer_load_dwordx4 v220, s[40:43], s54 offen offset:1024 nt lds\n"
    "v_add_f32 v184, v184, v181\n"
    "v_exp_f32 v182, v182\n"
    "v_mfma_f32_32x32x16_bf16 a[176:191], a[148:151], v[164:167], a[176:191]\n"
    "s_add_u32 m0, s56, 8192\n"
    "ds_read_b64_tr_b16 a[136:137], v212 offset:16384\n"
    "v_fma_f32 v183, v159, s52, v188\n"
    "v_add_f32 v184, v184, v182\n"
    "v_exp_f32 v183, v183\n"
    "v_cvt_pk_bf16_f32 v174, v180, v181\n"
    "v_mfma_f32_32x32x16_bf16 a[192:207], a[152:155], v[164:167], a[192:207]\n"
    "ds_read_b64_tr_b16 a[138:139], v212 offset:18432\n"
    "buffer_load_dwordx4 v218, s[36:39], s63 offen nt lds\n"
    "v_add_f32 v184, v184, v183\n"
    "v_cvt_pk_bf16_f32 v175, v182, v183\n"
    "v_cmp_nge_f32 vcc, s53, v184\n"
    "v_mfma_f32_32x32x16_bf16 a[208:223], a[156:159], v[164:167], a[208:223]\n"
    "s_cbranch_vccnz rare_stub_39\n"
    "back_39:\n"
    "ds_read_b64_tr_b16 a[140:141], v213 offset:16384\n"
    "v_add_f32 v185, v185, v184\n"
    "v_mfma_f32_32x32x16_bf16 v[144:159], a[96:99], a[64:67], 0\n"
    "s_mov_b32 s62, 4\n"
    "loop_1:\n"
    "ds_read_b64_tr_b16 a[142:143], v213 offset:18432\n"
    "buffer_load_dwordx4 v219, s[36:39], s63 offen offset:1024 nt lds\n"
    "v_fma_f32 v176, v128, s52, v189\n"
    "v_exp_f32 v176, v176\n"
    "v_mfma_f32_32x32x16_bf16 v[144:159], a[100:103], a[68:71], v[144:159]\n"
    "ds_read_b64_tr_b16 a[144:145], v210 offset:20480\n"
    "v_fma_f32 v177, v129, s52, v189\n"
    "v_exp_f32 v177, v177\n"
    "v_fma_f32 v178, v130, s52, v189\n"
    "v_add_f32 v184, v176, v177\n"
    "v_mfma_f32_32x32x16_bf16 v[144:159], a[104:107], a[72:75], v[144:159]\n"
    "ds_read_b64_tr_b16 a[146:147], v210 offset:22528\n"
    "v_cvt_pk_bf16_f32 v160, v176, v177\n"
    "v_exp_f32 v178, v178\n"
    "v_fma_f32 v179, v131, s52, v189\n"
    "v_add_f32 v184, v184, v178\n"
    "v_mfma_f32_32x32x16_bf16 v[144:159], a[108:111], a[76:79], v[144:159]\n"
    "ds_read_b64_tr_b16 a[148:149], v211 offset:20480\n"
    "v_exp_f32 v179, v179\n"
    "v_fma_f32 v180, v132, s52, v189\n"
    "v_add_f32 v184, v184, v179\n"
    "v_cvt_pk_bf16_f32 v161, v178, v179\n"
    "v_mfma_f32_32x32x16_bf16 v[144:159], a[112:115], a[80:83], v[144:159]\n"
    "ds_read_b64_tr_b16 a[150:151], v211 offset:22528\n"
    "v_exp_f32 v180, v180\n"
    "v_fma_f32 v181, v133, s52, v189\n"
    "v_add_f32 v184, v184, v180\n"
    "v_exp_f32 v181, v181\n"
    "v_mfma_f32_32x32x16_bf16 v[144:159], a[116:119], a[84:87], v[144:159]\n"
    "ds_read_b64_tr_b16 a[152:153], v212 offset:20480\n"
    "v_fma_f32 v182, v134, s52, v189\n"
    "v_add_f32 v184, v184, v181\n"
    "v_cvt_pk_bf16_f32 v162, v180, v181\n"
    "v_exp_f32 v182, v182\n"
    "v_mfma_f32_32x32x16_bf16 v[144:159], a[120:123], a[88:91], v[144:159]\n"
    "ds_read_b64_tr_b16 a[154:155], v212 offset:22528\n"
    "v_fma_f32 v183, v135, s52, v189\n"
    "v_add_f32 v184, v184, v182\n"
    "v_exp_f32 v183, v183\n"
    "v_fma_f32 v176, v136, s52, v189\n"
    "v_mfma_f32_32x32x16_bf16 v[144:159], a[124:127], a[92:95], v[144:159]\n"
    "s_waitcnt lgkmcnt(3)\n"
    "ds_read_b64_tr_b16 a[156:157], v213 offset:20480\n"
    "v_add_f32 v184, v184, v183\n"
    "v_cvt_pk_bf16_f32 v163, v182, v183\n"
    "v_exp_f32 v176, v176\n"
    "v_fma_f32 v177, v137, s52, v189\n"
    "v_mfma_f32_32x32x16_bf16 v[0:15], a[128:131], v[168:171], v[0:15]\n"
    "ds_read_b64_tr_b16 a[158:159], v213 offset:22528\n"
    "ds_read_b128 a[96:99], v194 offset:24576\n"
    "v_add_f32 v184, v184, v176\n"
    "v_exp_f32 v177, v177\n"
    "v_fma_f32 v178, v138, s52, v189\n"
    "v_mfma_f32_32x32x16_bf16 v[16:31], a[132:135], v[168:171], v[16:31]\n"
    "ds_read_b128 a[100:103], v195 offset:24576\n"
    "v_add_f32 v184, v184, v177\n"
    "v_cvt_pk_bf16_f32 v164, v176, v177\n"
    "v_exp_f32 v178, v178\n"
    "v_fma_f32 v179, v139, s52, v189\n"
    "v_mfma_f32_32x32x16_bf16 v[32:47], a[136:139], v[168:171], v[32:47]\n"
    "ds_read_b128 a[104:107], v196 offset:24576\n"
    "v_add_f32 v184, v184, v178\n"
    "v_exp_f32 v179, v179\n"
    "v_fma_f32 v180, v140, s52, v189\n"
    "v_add_f32 v184, v184, v179\n"
    "v_mfma_f32_32x32x16_bf16 v[48:63], a[140:143], v[168:171], v[48:63]\n"
    "ds_read_b128 a[108:111], v197 offset:24576\n"
    "v_cvt_pk_bf16_f32 v165, v178, v179\n"
    "v_exp_f32 v180, v180\n"
    "v_fma_f32 v181, v141, s52, v189\n"
    "v_add_f32 v184, v184, v180\n"
    "v_mfma_f32_32x32x16_bf16 v[0:15], a[144:147], v[172:175], v[0:15]\n"
    "s_waitcnt lgkmcnt(3)\n"
    "ds_read_b128 a[112:115], v198 offset:24576\n"
    "v_exp_f32 v181, v181\n"
    "v_fma_f32 v182, v142, s52, v189\n"
    "v_add_f32 v184, v184, v181\n"
    "v_xor_b32 v210, 0x8000, v210\n"
    "v_mfma_f32_32x32x16_bf16 v[16:31], a[148:151], v[172:175], v[16:31]\n"
    "ds_read_b128 a[116:119], v199 offset:24576\n"
    "v_exp_f32 v182, v182\n"
    "v_fma_f32 v183, v143, s52, v189\n"
    "v_add_f32 v184, v184, v182\n"
    "v_exp_f32 v183, v183\n"
    "v_mfma_f32_32x32x16_bf16 v[32:47], a[152:155], v[172:175], v[32:47]\n"
    "ds_read_b128 a[120:123], v200 offset:24576\n"
    "v_cvt_pk_bf16_f32 v166, v180, v181\n"
    "v_add_f32 v184, v184, v183\n"
    "v_cvt_pk_bf16_f32 v167, v182, v183\n"
    "v_cmp_nge_f32 vcc, s53, v184\n"
    "v_xor_b32 v211, 0x8000, v211\n"
    "v_mfma_f32_32x32x16_bf16 v[48:63], a[156:159], v[172:175], v[48:63]\n"
    "s_cbranch_vccnz rare_stub_40\n"
    "back_40:\n"
    "ds_read_b128 a[124:127], v201 offset:24576\n"
    "v_add_f32 v186, v186, v184\n"
    "v_xor_b32 v212, 0x8000, v212\n"
    "v_mfma_f32_32x32x16_bf16 v[128:143], a[96:99], a[0:3], 0\n"
    "s_waitcnt lgkmcnt(3)\n"
    "v_fma_f32 v176, v144, s52, v190\n"
    "v_exp_f32 v176, v176\n"
    "v_fma_f32 v177, v145, s52, v190\n"
    "v_exp_f32 v177, v177\n"
    "v_mfma_f32_32x32x16_bf16 v[128:143], a[100:103], a[4:7], v[128:143]\n"
    "s_mov_b32 s54, s63\n"
    "v_fma_f32 v178, v146, s52, v190\n"
    "v_add_f32 v184, v176, v177\n"
    "v_cvt_pk_bf16_f32 v168, v176, v177\n"
    "v_exp_f32 v178, v178\n"
    "v_xor_b32 v213, 0x8000, v213\n"
    "v_mfma_f32_32x32x16_bf16 v[128:143], a[104:107], a[8:11], v[128:143]\n"
    "s_add_u32 s63, s63, 8192\n"
    "v_add_u32 v222, 0x1000, v222\n"
    "v_fma_f32 v179, v147, s52, v190\n"
    "v_add_f32 v184, v184, v178\n"
    "v_exp_f32 v179, v179\n"
    "v_fma_f32 v180, v148, s52, v190\n"
    "v_mfma_f32_32x32x16_bf16 v[128:143], a[108:111], a[12:15], v[128:143]\n"
    "v_add_f32 v184, v184, v179\n"
    "v_cvt_pk_bf16_f32 v169, v178, v179\n"
    "v_exp_f32 v180, v180\n"
    "v_fma_f32 v181, v149, s52, v190\n"
    "v_xor_b32 v194, 0x8000, v194\n"
    "v_mfma_f32_32x32x16_bf16 v[128:143], a[112:115], a[16:19], v[128:143]\n"
    "s_waitcnt lgkmcnt(0)\n"
    "v_add_f32 v184, v184, v180\n"
    "v_exp_f32 v181, v181\n"
    "v_fma_f32 v182, v150, s52, v190\n"
    "v_add_f32 v184, v184, v181\n"
    "v_xor_b32 v195, 0x8000, v195\n"
    "v_mfma_f32_32x32x16_bf16 v[128:143], a[116:119], a[20:23], v[128:143]\n"
    "v_cvt_pk_bf16_f32 v170, v180, v181\n"
    "v_exp_f32 v182, v182\n"
    "v_fma_f32 v183, v151, s52, v190\n"
    "v_add_f32 v184, v184, v182\n"
    "v_xor_b32 v196, 0x8000, v196\n"
    "v_mfma_f32_32x32x16_bf16 v[128:143], a[120:123], a[24:27], v[128:143]\n"
    "s_add_u32 s55, s55, 4096\n"
    "ds_read_b128 v[240:243], v222 offset:0\n"
    "v_exp_f32 v183, v183\n"
    "v_fma_f32 v176, v152, s52, v190\n"
    "v_add_f32 v184, v184, v183\n"
    "v_cvt_pk_bf16_f32 v171, v182, v183\n"
    "v_mfma_f32_32x32x16_bf16 v[128:143], a[124:127], a[28:31], v[128:143]\n"
    "v_exp_f32 v176, v176\n"
    "v_fma_f32 v177, v153, s52, v190\n"
    "v_add_f32 v184, v184, v176\n"
    "v_exp_f32 v177, v177\n"
    "v_mfma_f32_32x32x16_bf16 v[64:79], a[128:131], v[160:163], v[64:79]\n"
    "v_fma_f32 v178, v154, s52, v190\n"
    "v_add_f32 v184, v184, v177\n"
    "v_cvt_pk_bf16_f32 v172, v176, v177\n"
    "v_exp_f32 v178, v178\n"
    "v_xor_b32 v197, 0x8000, v197\n"
    "v_mfma_f32_32x32x16_bf16 v[80:95], a[132:135], v[160:163], v[80:95]\n"
    "s_waitcnt lgkmcnt(0)\n"
    "buffer_store_dwordx4 v[240:243], v214, s[48:51], s55 offen nt\n"
    "v_fma_f32 v179, v155, s52, v190\n"
    "v_add_f32 v184, v184, v178\n"
    "v_mfma_f32_32x32x16_bf16 v[96:111], a[136:139], v[160:163], v[96:111]\n"
    "ds_read_b128 v[244:247], v222 offset:1024\n"
    "v_exp_f32 v179, v179\n"
    "v_fma_f32 v180, v156, s52, v190\n"
    "v_add_f32 v184, v184, v179\n"
    "v_cvt_pk_bf16_f32 v173, v178, v179\n"
    "v_mfma_f32_32x32x16_bf16 v[112:127], a[140:143], v[160:163], v[112:127]\n"
    "v_exp_f32 v180, v180\n"
    "v_fma_f32 v181, v157, s52, v190\n"
    "v_add_f32 v184, v184, v180\n"
    "v_exp_f32 v181, v181\n"
    "v_mfma_f32_32x32x16_bf16 v[64:79], a[144:147], v[164:167], v[64:79]\n"
    "v_fma_f32 v182, v158, s52, v190\n"
    "v_add_f32 v184, v184, v181\n"
    "v_exp_f32 v182, v182\n"
    "v_fma_f32 v183, v159, s52, v190\n"
    "v_xor_b32 v198, 0x8000, v198\n"
    "v_mfma_f32_32x32x16_bf16 v[80:95], a[148:151], v[164:167], v[80:95]\n"
    "v_add_f32 v184, v184, v182\n"
    "v_exp_f32 v183, v183\n"
    "v_cvt_pk_bf16_f32 v174, v180, v181\n"
    "v_add_f32 v184, v184, v183\n"
    "v_xor_b32 v199, 0x8000, v199\n"
    "v_mfma_f32_32x32x16_bf16 v[96:111], a[152:155], v[164:167], v[96:111]\n"
    "s_waitcnt lgkmcnt(0)\n"
    "buffer_store_dwordx4 v[244:247], v215, s[48:51], s55 offen offset:1024 nt\n"
    "v_cvt_pk_bf16_f32 v175, v182, v183\n"
    "v_cmp_nge_f32 vcc, s53, v184\n"
    "v_mfma_f32_32x32x16_bf16 v[112:127], a[156:159], v[164:167], v[112:127]\n"
    "s_cbranch_vccnz rare_stub_41\n"
    "back_41:\n"
    "v_add_f32 v187, v187, v184\n"
    "v_xor_b32 v200, 0x8000, v200\n"
    "v_xor_b32 v201, 0x8000, v201\n"
    "v_mfma_f32_32x32x16_bf16 v[144:159], a[96:99], a[32:35], 0\n"
    "v_fma_f32 v176, v128, s52, v188\n"
    "v_exp_f32 v176, v176\n"
    "v_fma_f32 v177, v129, s52, v188\n"
    "v_exp_f32 v177, v177\n"
    "v_mfma_f32_32x32x16_bf16 v[144:159], a[100:103], a[36:39], v[144:159]\n"
    "v_fma_f32 v178, v130, s52, v188\n"
    "v_add_f32 v184, v176, v177\n"
    "v_cvt_pk_bf16_f32 v160, v176, v177\n"
    "v_exp_f32 v178, v178\n"
    "v_fma_f32 v179, v131, s52, v188\n"
    "v_mfma_f32_32x32x16_bf16 v[144:159], a[104:107], a[40:43], v[144:159]\n"
    "v_add_f32 v184, v184, v178\n"
    "v_exp_f32 v179, v179\n"
    "v_fma_f32 v180, v132, s52, v188\n"
    "v_add_f32 v184, v184, v179\n"
    "v_cvt_pk_bf16_f32 v161, v178, v179\n"
    "v_mfma_f32_32x32x16_bf16 v[144:159], a[108:111], a[44:47], v[144:159]\n"
    "v_exp_f32 v180, v180\n"
    "v_fma_f32 v181, v133, s52, v188\n"
    "v_add_f32 v184, v184, v180\n"
    "v_exp_f32 v181, v181\n"
    "v_mfma_f32_32x32x16_bf16 v[144:159], a[112:115], a[48:51], v[144:159]\n"
    "v_fma_f32 v182, v134, s52, v188\n"
    "v_add_f32 v184, v184, v181\n"
    "v_cvt_pk_bf16_f32 v162, v180, v181\n"
    "v_exp_f32 v182, v182\n"
    "v_fma_f32 v183, v135, s52, v188\n"
    "v_mfma_f32_32x32x16_bf16 v[144:159], a[116:119], a[52:55], v[144:159]\n"
    "v_add_f32 v184, v184, v182\n"
    "v_exp_f32 v183, v183\n"
    "v_fma_f32 v176, v136, s52, v188\n"
    "v_add_f32 v184, v184, v183\n"
    "v_cvt_pk_bf16_f32 v163, v182, v183\n"
    "v_mfma_f32_32x32x16_bf16 v[144:159], a[120:123], a[56:59], v[144:159]\n"
    "v_exp_f32 v176, v176\n"
    "v_fma_f32 v177, v137, s52, v188\n"
    "v_add_f32 v184, v184, v176\n"
    "v_exp_f32 v177, v177\n"
    "v_mfma_f32_32x32x16_bf16 v[144:159], a[124:127], a[60:63], v[144:159]\n"
    "v_fma_f32 v178, v138, s52, v188\n"
    "v_add_f32 v184, v184, v177\n"
    "v_cvt_pk_bf16_f32 v164, v176, v177\n"
    "v_exp_f32 v178, v178\n"
    "v_fma_f32 v179, v139, s52, v188\n"
    "v_mfma_f32_32x32x16_bf16 a[160:175], a[128:131], v[168:171], a[160:175]\n"
    "s_waitcnt vmcnt(14)\n"
    "s_barrier\n"
    "v_add_f32 v184, v184, v178\n"
    "v_mfma_f32_32x32x16_bf16 a[176:191], a[132:135], v[168:171], a[176:191]\n"
    "ds_read_b64_tr_b16 a[128:129], v210 offset:0\n"
    "v_exp_f32 v179, v179\n"
    "v_fma_f32 v180, v140, s52, v188\n"
    "v_add_f32 v184, v184, v179\n"
    "v_cvt_pk_bf16_f32 v165, v178, v179\n"
    "v_mfma_f32_32x32x16_bf16 a[192:207], a[136:139], v[168:171], a[192:207]\n"
    "s_add_u32 m0, s56, 16384\n"
    "ds_read_b64_tr_b16 a[130:131], v210 offset:2048\n"
    "buffer_load_dwordx4 v220, s[40:43], s54 offen nt lds\n"
    "v_exp_f32 v180, v180\n"
    "v_fma_f32 v181, v141, s52, v188\n"
    "v_mfma_f32_32x32x16_bf16 a[208:223], a[140:143], v[168:171], a[208:223]\n"
    "ds_read_b64_tr_b16 a[132:133], v211 offset:0\n"
    "v_add_f32 v184, v184, v180\n"
    "v_exp_f32 v181, v181\n"
    "v_fma_f32 v182, v142, s52, v188\n"
    "v_add_f32 v184, v184, v181\n"
    "v_mfma_f32_32x32x16_bf16 a[160:175], a[144:147], v[172:175], a[160:175]\n"
    "ds_read_b64_tr_b16 a[134:135], v211 offset:2048\n"
    "buffer_load_dwordx4 v220, s[40:43], s54 offen offset:1024 nt lds\n"
    "v_exp_f32 v182, v182\n"
    "v_fma_f32 v183, v143, s52, v188\n"
    "v_mfma_f32_32x32x16_bf16 a[176:191], a[148:151], v[172:175], a[176:191]\n"
    "s_add_u32 m0, s56, 24576\n"
    "ds_read_b64_tr_b16 a[136:137], v212 offset:0\n"
    "v_add_f32 v184, v184, v182\n"
    "v_exp_f32 v183, v183\n"
    "v_cvt_pk_bf16_f32 v166, v180, v181\n"
    "v_mfma_f32_32x32x16_bf16 a[192:207], a[152:155], v[172:175], a[192:207]\n"
    "ds_read_b64_tr_b16 a[138:139], v212 offset:2048\n"
    "buffer_load_dwordx4 v218, s[36:39], s63 offen nt lds\n"
    "v_add_f32 v184, v184, v183\n"
    "v_cvt_pk_bf16_f32 v167, v182, v183\n"
    "v_cmp_nge_f32 vcc, s53, v184\n"
    "v_mfma_f32_32x32x16_bf16 a[208:223], a[156:159], v[172:175], a[208:223]\n"
    "s_cbranch_vccnz rare_stub_42\n"
    "back_42:\n"
    "ds_read_b64_tr_b16 a[140:141], v213 offset:0\n"
    "v_add_f32 v185, v185, v184\n"
    "v_mfma_f32_32x32x16_bf16 v[128:143], a[96:99], a[64:67], 0\n"
    "ds_read_b64_tr_b16 a[142:143], v213 offset:2048\n"
    "buffer_load_dwordx4 v219, s[36:39], s63 offen offset:1024 nt lds\n"
    "v_fma_f32 v176, v144, s52, v189\n"
    "v_exp_f32 v176, v176\n"
    "v_mfma_f32_32x32x16_bf16 v[128:143], a[100:103], a[68:71], v[128:143]\n"
    "ds_read_b64_tr_b16 a[144:145], v210 offset:4096\n"
    "v_fma_f32 v177, v145, s52, v189\n"
    "v_exp_f32 v177, v177\n"
    "v_fma_f32 v178, v146, s52, v189\n"
    "v_add_f32 v184, v176, v177\n"
    "v_mfma_f32_32x32x16_bf16 v[128:143], a[104:107], a[72:75], v[128:143]\n"
    "ds_read_b64_tr_b16 a[146:147], v210 offset:6144\n"
    "v_cvt_pk_bf16_f32 v168, v176, v177\n"
    "v_exp_f32 v178, v178\n"
    "v_fma_f32 v179, v147, s52, v189\n"
    "v_add_f32 v184, v184, v178\n"
    "v_mfma_f32_32x32x16_bf16 v[128:143], a[108:111], a[76:79], v[128:143]\n"
    "ds_read_b64_tr_b16 a[148:149], v211 offset:4096\n"
    "v_exp_f32 v179, v179\n"
    "v_fma_f32 v180, v148, s52, v189\n"
    "v_add_f32 v184, v184, v179\n"
    "v_cvt_pk_bf16_f32 v169, v178, v179\n"
    "v_mfma_f32_32x32x16_bf16 v[128:143], a[112:115], a[80:83], v[128:143]\n"
    "ds_read_b64_tr_b16 a[150:151], v211 offset:6144\n"
    "v_exp_f32 v180, v180\n"
    "v_fma_f32 v181, v149, s52, v189\n"
    "v_add_f32 v184, v184, v180\n"
    "v_exp_f32 v181, v181\n"
    "v_mfma_f32_32x32x16_bf16 v[128:143], a[116:119], a[84:87], v[128:143]\n"
    "ds_read_b64_tr_b16 a[152:153], v212 offset:4096\n"
    "v_fma_f32 v182, v150, s52, v189\n"
    "v_add_f32 v184, v184, v181\n"
    "v_cvt_pk_bf16_f32 v170, v180, v181\n"
    "v_exp_f32 v182, v182\n"
    "v_mfma_f32_32x32x16_bf16 v[128:143], a[120:123], a[88:91], v[128:143]\n"
    "ds_read_b64_tr_b16 a[154:155], v212 offset:6144\n"
    "v_fma_f32 v183, v151, s52, v189\n"
    "v_add_f32 v184, v184, v182\n"
    "v_exp_f32 v183, v183\n"
    "v_fma_f32 v176, v152, s52, v189\n"
    "v_mfma_f32_32x32x16_bf16 v[128:143], a[124:127], a[92:95], v[128:143]\n"
    "s_waitcnt lgkmcnt(3)\n"
    "ds_read_b64_tr_b16 a[156:157], v213 offset:4096\n"
    "v_add_f32 v184, v184, v183\n"
    "v_cvt_pk_bf16_f32 v171, v182, v183\n"
    "v_exp_f32 v176, v176\n"
    "v_fma_f32 v177, v153, s52, v189\n"
    "v_mfma_f32_32x32x16_bf16 v[0:15], a[128:131], v[160:163], v[0:15]\n"
    "ds_read_b64_tr_b16 a[158:159], v213 offset:6144\n"
    "ds_read_b128 a[96:99], v194 offset:8192\n"
    "v_add_f32 v184, v184, v176\n"
    "v_exp_f32 v177, v177\n"
    "v_fma_f32 v178, v154, s52, v189\n"
    "v_mfma_f32_32x32x16_bf16 v[16:31], a[132:135], v[160:163], v[16:31]\n"
    "ds_read_b128 a[100:103], v195 offset:8192\n"
    "v_add_f32 v184, v184, v177\n"
    "v_cvt_pk_bf16_f32 v172, v176, v177\n"
    "v_exp_f32 v178, v178\n"
    "v_fma_f32 v179, v155, s52, v189\n"
    "v_mfma_f32_32x32x16_bf16 v[32:47], a[136:139], v[160:163], v[32:47]\n"
    "ds_read_b128 a[104:107], v196 offset:8192\n"
    "v_add_f32 v184, v184, v178\n"
    "v_exp_f32 v179, v179\n"
    "v_fma_f32 v180, v156, s52, v189\n"
    "v_add_f32 v184, v184, v179\n"
    "v_mfma_f32_32x32x16_bf16 v[48:63], a[140:143], v[160:163], v[48:63]\n"
    "ds_read_b128 a[108:111], v197 offset:8192\n"
    "v_cvt_pk_bf16_f32 v173, v178, v179\n"
    "v_exp_f32 v180, v180\n"
    "v_fma_f32 v181, v157, s52, v189\n"
    "v_add_f32 v184, v184, v180\n"
    "v_mfma_f32_32x32x16_bf16 v[0:15], a[144:147], v[164:167], v[0:15]\n"
    "s_waitcnt lgkmcnt(3)\n"
    "ds_read_b128 a[112:115], v198 offset:8192\n"
    "v_exp_f32 v181, v181\n"
    "v_fma_f32 v182, v158, s52, v189\n"
    "v_add_f32 v184, v184, v181\n"
    "v_mfma_f32_32x32x16_bf16 v[16:31], a[148:151], v[164:167], v[16:31]\n"
    "ds_read_b128 a[116:119], v199 offset:8192\n"
    "v_exp_f32 v182, v182\n"
    "v_fma_f32 v183, v159, s52, v189\n"
    "v_add_f32 v184, v184, v182\n"
    "v_exp_f32 v183, v183\n"
    "v_mfma_f32_32x32x16_bf16 v[32:47], a[152:155], v[164:167], v[32:47]\n"
    "ds_read_b128 a[120:123], v200 offset:8192\n"
    "v_cvt_pk_bf16_f32 v174, v180, v181\n"
    "v_add_f32 v184, v184, v183\n"
    "v_cvt_pk_bf16_f32 v175, v182, v183\n"
    "v_cmp_nge_f32 vcc, s53, v184\n"
    "v_mfma_f32_32x32x16_bf16 v[48:63], a[156:159], v[164:167], v[48:63]\n"
    "s_cbranch_vccnz rare_stub_43\n"
    "back_43:\n"
    "ds_read_b128 a[124:127], v201 offset:8192\n"
    "v_add_f32 v186, v186, v184\n"
    "v_mfma_f32_32x32x16_bf16 v[144:159], a[96:99], a[0:3], 0\n"
    "s_waitcnt lgkmcnt(3)\n"
    "v_fma_f32 v176, v128, s52, v190\n"
    "v_exp_f32 v176, v176\n"
    "v_fma_f32 v177, v129, s52, v190\n"
    "v_exp_f32 v177, v177\n"
    "v_mfma_f32_32x32x16_bf16 v[144:159], a[100:103], a[4:7], v[144:159]\n"
    "s_mov_b32 s54, s63\n"
    "v_fma_f32 v178, v130, s52, v190\n"
    "v_add_f32 v184, v176, v177\n"
    "v_cvt_pk_bf16_f32 v160, v176, v177\n"
    "v_exp_f32 v178, v178\n"
    "v_mfma_f32_32x32x16_bf16 v[144:159], a[104:107], a[8:11], v[144:159]\n"
    "s_add_u32 s63, s63, 8192\n"
    "v_fma_f32 v179, v131, s52, v190\n"
    "v_add_f32 v184, v184, v178\n"
    "v_exp_f32 v179, v179\n"
    "v_fma_f32 v180, v132, s52, v190\n"
    "v_mfma_f32_32x32x16_bf16 v[144:159], a[108:111], a[12:15], v[144:159]\n"
    "s_xor_b32 s56, s56, 32768\n"
    "v_add_f32 v184, v184, v179\n"
    "v_cvt_pk_bf16_f32 v161, v178, v179\n"
    "v_exp_f32 v180, v180\n"
    "v_fma_f32 v181, v133, s52, v190\n"
    "v_mfma_f32_32x32x16_bf16 v[144:159], a[112:115], a[16:19], v[144:159]\n"
    "s_waitcnt lgkmcnt(0)\n"
    "v_add_f32 v184, v184, v180\n"
    "v_exp_f32 v181, v181\n"
    "v_fma_f32 v182, v134, s52, v190\n"
    "v_add_f32 v184, v184, v181\n"
    "v_mfma_f32_32x32x16_bf16 v[144:159], a[116:119], a[20:23], v[144:159]\n"
    "v_cvt_pk_bf16_f32 v162, v180, v181\n"
    "v_exp_f32 v182, v182\n"
    "v_fma_f32 v183, v135, s52, v190\n"
    "v_add_f32 v184, v184, v182\n"
    "v_mfma_f32_32x32x16_bf16 v[144:159], a[120:123], a[24:27], v[144:159]\n"
    "ds_read_b128 v[240:243], v222 offset:2048\n"
    "v_exp_f32 v183, v183\n"
    "v_fma_f32 v176, v136, s52, v190\n"
    "v_add_f32 v184, v184, v183\n"
    "v_cvt_pk_bf16_f32 v163, v182, v183\n"
    "v_mfma_f32_32x32x16_bf16 v[144:159], a[124:127], a[28:31], v[144:159]\n"
    "v_exp_f32 v176, v176\n"
    "v_fma_f32 v177, v137, s52, v190\n"
    "v_add_f32 v184, v184, v176\n"
    "v_exp_f32 v177, v177\n"
    "v_mfma_f32_32x32x16_bf16 v[64:79], a[128:131], v[168:171], v[64:79]\n"
    "v_fma_f32 v178, v138, s52, v190\n"
    "v_add_f32 v184, v184, v177\n"
    "v_cvt_pk_bf16_f32 v164, v176, v177\n"
    "v_exp_f32 v178, v178\n"
    "v_mfma_f32_32x32x16_bf16 v[80:95], a[132:135], v[168:171], v[80:95]\n"
    "s_waitcnt lgkmcnt(0)\n"
    "buffer_store_dwordx4 v[240:243], v216, s[48:51], s55 offen offset:2048 nt\n"
    "v_fma_f32 v179, v139, s52, v190\n"
    "v_add_f32 v184, v184, v178\n"
    "v_mfma_f32_32x32x16_bf16 v[96:111], a[136:139], v[168:171], v[96:111]\n"
    "ds_read_b128 v[244:247], v222 offset:3072\n"
    "v_exp_f32 v179, v179\n"
    "v_fma_f32 v180, v140, s52, v190\n"
    "v_add_f32 v184, v184, v179\n"
    "v_cvt_pk_bf16_f32 v165, v178, v179\n"
    "v_mfma_f32_32x32x16_bf16 v[112:127], a[140:143], v[168:171], v[112:127]\n"
    "v_exp_f32 v180, v180\n"
    "v_fma_f32 v181, v141, s52, v190\n"
    "v_add_f32 v184, v184, v180\n"
    "v_exp_f32 v181, v181\n"
    "v_mfma_f32_32x32x16_bf16 v[64:79], a[144:147], v[172:175], v[64:79]\n"
    "v_fma_f32 v182, v142, s52, v190\n"
    "v_add_f32 v184, v184, v181\n"
    "v_exp_f32 v182, v182\n"
    "v_fma_f32 v183, v143, s52, v190\n"
    "v_mfma_f32_32x32x16_bf16 v[80:95], a[148:151], v[172:175], v[80:95]\n"
    "v_add_f32 v184, v184, v182\n"
    "v_exp_f32 v183, v183\n"
    "v_cvt_pk_bf16_f32 v166, v180, v181\n"
    "v_add_f32 v184, v184, v183\n"
    "v_mfma_f32_32x32x16_bf16 v[96:111], a[152:155], v[172:175], v[96:111]\n"
    "s_waitcnt lgkmcnt(0)\n"
    "buffer_store_dwordx4 v[244:247], v217, s[48:51], s55 offen offset:3072 nt\n"
    "v_cvt_pk_bf16_f32 v167, v182, v183\n"
    "v_cmp_nge_f32 vcc, s53, v184\n"
    "v_mfma_f32_32x32x16_bf16 v[112:127], a[156:159], v[172:175], v[112:127]\n"
    "s_cbranch_vccnz rare_stub_44\n"
    "back_44:\n"
    "v_add_f32 v187, v187, v184\n"
    "v_mfma_f32_32x32x16_bf16 v[128:143], a[96:99], a[32:35], 0\n"
    "v_fma_f32 v176, v144, s52, v188\n"
    "v_exp_f32 v176, v176\n"
    "v_fma_f32 v177, v145, s52, v188\n"
    "v_exp_f32 v177, v177\n"
    "v_mfma_f32_32x32x16_bf16 v[128:143], a[100:103], a[36:39], v[128:143]\n"
    "v_fma_f32 v178, v146, s52, v188\n"
    "v_add_f32 v184, v176, v177\n"
    "v_cvt_pk_bf16_f32 v168, v176, v177\n"
    "v_exp_f32 v178, v178\n"
    "v_fma_f32 v179, v147, s52, v188\n"
    "v_mfma_f32_32x32x16_bf16 v[128:143], a[104:107], a[40:43], v[128:143]\n"
    "v_add_f32 v184, v184, v178\n"
    "v_exp_f32 v179, v179\n"
    "v_fma_f32 v180, v148, s52, v188\n"
    "v_add_f32 v184, v184, v179\n"
    "v_cvt_pk_bf16_f32 v169, v178, v179\n"
    "v_mfma_f32_32x32x16_bf16 v[128:143], a[108:111], a[44:47], v[128:143]\n"
    "v_exp_f32 v180, v180\n"
    "v_fma_f32 v181, v149, s52, v188\n"
    "v_add_f32 v184, v184, v180\n"
    "v_exp_f32 v181, v181\n"
    "v_mfma_f32_32x32x16_bf16 v[128:143], a[112:115], a[48:51], v[128:143]\n"
    "v_fma_f32 v182, v150, s52, v188\n"
    "v_add_f32 v184, v184, v181\n"
    "v_cvt_pk_bf16_f32 v170, v180, v181\n"
    "v_exp_f32 v182, v182\n"
    "v_fma_f32 v183, v151, s52, v188\n"
    "v_mfma_f32_32x32x16_bf16 v[128:143], a[116:119], a[52:55], v[128:143]\n"
    "v_add_f32 v184, v184, v182\n"
    "v_exp_f32 v183, v183\n"
    "v_fma_f32 v176, v152, s52, v188\n"
    "v_add_f32 v184, v184, v183\n"
    "v_cvt_pk_bf16_f32 v171, v182, v183\n"
    "v_mfma_f32_32x32x16_bf16 v[128:143], a[120:123], a[56:59], v[128:143]\n"
    "v_exp_f32 v176, v176\n"
    "v_fma_f32 v177, v153, s52, v188\n"
    "v_add_f32 v184, v184, v176\n"
    "v_exp_f32 v177, v177\n"
    "v_mfma_f32_32x32x16_bf16 v[128:143], a[124:127], a[60:63], v[128:143]\n"
    "v_fma_f32 v178, v154, s52, v188\n"
    "v_add_f32 v184, v184, v177\n"
    "v_cvt_pk_bf16_f32 v172, v176, v177\n"
    "v_exp_f32 v178, v178\n"
    "v_fma_f32 v179, v155, s52, v188\n"
    "v_mfma_f32_32x32x16_bf16 a[160:175], a[128:131], v[160:163], a[160:175]\n"
    "s_waitcnt vmcnt(14)\n"
    "s_barrier\n"
    "v_add_f32 v184, v184, v178\n"
    "v_mfma_f32_32x32x16_bf16 a[176:191], a[132:135], v[160:163], a[176:191]\n"
    "ds_read_b64_tr_b16 a[128:129], v210 offset:16384\n"
    "v_exp_f32 v179, v179\n"
    "v_fma_f32 v180, v156, s52, v188\n"
    "v_add_f32 v184, v184, v179\n"
    "v_cvt_pk_bf16_f32 v173, v178, v179\n"
    "v_mfma_f32_32x32x16_bf16 a[192:207], a[136:139], v[160:163], a[192:207]\n"
    "s_add_u32 m0, s56, 0\n"
    "ds_read_b64_tr_b16 a[130:131], v210 offset:18432\n"
    "buffer_load_dwordx4 v220, s[40:43], s54 offen nt lds\n"
    "v_exp_f32 v180, v180\n"
    "v_fma_f32 v181, v157, s52, v188\n"
    "v_mfma_f32_32x32x16_bf16 a[208:223], a[140:143], v[160:163], a[208:223]\n"
    "ds_read_b64_tr_b16 a[132:133], v211 offset:16384\n"
    "v_add_f32 v184, v184, v180\n"
    "v_exp_f32 v181, v181\n"
    "v_fma_f32 v182, v158, s52, v188\n"
    "v_add_f32 v184, v184, v181\n"
    "v_mfma_f32_32x32x16_bf16 a[160:175], a[144:147], v[164:167], a[160:175]\n"
    "ds_read_b64_tr_b16 a[134:135], v211 offset:18432\n"
    "buffer_load_dwordx4 v220, s[40:43], s54 offen offset:1024 nt lds\n"
    "v_exp_f32 v182, v182\n"
    "v_fma_f32 v183, v159, s52, v188\n"
    "v_mfma_f32_32x32x16_bf16 a[176:191], a[148:151], v[164:167], a[176:191]\n"
    "s_add_u32 m0, s56, 8192\n"
    "ds_read_b64_tr_b16 a[136:137], v212 offset:16384\n"
    "v_add_f32 v184, v184, v182\n"
    "v_exp_f32 v183, v183\n"
    "v_cvt_pk_bf16_f32 v174, v180, v181\n"
    "v_mfma_f32_32x32x16_bf16 a[192:207], a[152:155], v[164:167], a[192:207]\n"
    "ds_read_b64_tr_b16 a[138:139], v212 offset:18432\n"
    "buffer_load_dwordx4 v218, s[36:39], s63 offen nt lds\n"
    "v_add_f32 v184, v184, v183\n"
    "v_cvt_pk_bf16_f32 v175, v182, v183\n"
    "v_cmp_nge_f32 vcc, s53, v184\n"
    "v_mfma_f32_32x32x16_bf16 a[208:223], a[156:159], v[164:167], a[208:223]\n"
    "s_cbranch_vccnz rare_stub_45\n"
    "back_45:\n"
    "ds_read_b64_tr_b16 a[140:141], v213 offset:16384\n"
    "v_add_f32 v185, v185, v184\n"
    "v_mfma_f32_32x32x16_bf16 v[144:159], a[96:99], a[64:67], 0\n"
    "s_sub_u32 s62, s62, 1\n"
    "s_cbranch_scc0 loop_1\n"
    "ds_read_b64_tr_b16 a[142:143], v213 offset:18432\n"
    "buffer_load_dwordx4 v219, s[36:39], s63 offen offset:1024 nt lds\n"
    "v_mfma_f32_32x32x16_bf16 v[144:159], a[100:103], a[68:71], v[144:159]\n"
    "ds_read_b64_tr_b16 a[144:145], v210 offset:20480\n"
    "v_fma_f32 v176, v128, s52, v189\n"
    "v_exp_f32 v176, v176\n"
    "v_fma_f32 v177, v129, s52, v189\n"
    "v_mov_b32 v252, v185\n"
    "v_mfma_f32_32x32x16_bf16 v[144:159], a[104:107], a[72:75], v[144:159]\n"
    "ds_read_b64_tr_b16 a[146:147], v210 offset:22528\n"
    "v_exp_f32 v177, v177\n"
    "v_fma_f32 v178, v130, s52, v189\n"
    "v_add_f32 v184, v176, v177\n"
    "v_cvt_pk_bf16_f32 v160, v176, v177\n"
    "v_mfma_f32_32x32x16_bf16 v[144:159], a[108:111], a[76:79], v[144:159]\n"
    "ds_read_b64_tr_b16 a[148:149], v211 offset:20480\n"
    "v_exp_f32 v178, v178\n"
    "v_fma_f32 v179, v131, s52, v189\n"
    "v_add_f32 v184, v184, v178\n"
    "v_permlane32_swap_b32 v252, v185\n"
    "v_mfma_f32_32x32x16_bf16 v[144:159], a[112:115], a[80:83], v[144:159]\n"
    "ds_read_b64_tr_b16 a[150:151], v211 offset:22528\n"
    "v_exp_f32 v179, v179\n"
    "v_fma_f32 v180, v132, s52, v189\n"
    "v_add_f32 v184, v184, v179\n"
    "v_cvt_pk_bf16_f32 v161, v178, v179\n"
    "v_mfma_f32_32x32x16_bf16 v[144:159], a[116:119], a[84:87], v[144:159]\n"
    "ds_read_b64_tr_b16 a[152:153], v212 offset:20480\n"
    "v_exp_f32 v180, v180\n"
    "v_fma_f32 v181, v133, s52, v189\n"
    "v_add_f32 v184, v184, v180\n"
    "v_add_f32 v185, v185, v252\n"
    "v_mfma_f32_32x32x16_bf16 v[144:159], a[120:123], a[88:91], v[144:159]\n"
    "ds_read_b64_tr_b16 a[154:155], v212 offset:22528\n"
    "v_exp_f32 v181, v181\n"
    "v_fma_f32 v182, v134, s52, v189\n"
    "v_add_f32 v184, v184, v181\n"
    "v_cvt_pk_bf16_f32 v162, v180, v181\n"
    "v_mfma_f32_32x32x16_bf16 v[144:159], a[124:127], a[92:95], v[144:159]\n"
    "s_waitcnt lgkmcnt(3)\n"
    "ds_read_b64_tr_b16 a[156:157], v213 offset:20480\n"
    "v_exp_f32 v182, v182\n"
    "v_fma_f32 v183, v135, s52, v189\n"
    "v_add_f32 v184, v184, v182\n"
    "v_mfma_f32_32x32x16_bf16 v[0:15], a[128:131], v[168:171], v[0:15]\n"
    "ds_read_b64_tr_b16 a[158:159], v213 offset:22528\n"
    "v_exp_f32 v183, v183\n"
    "v_fma_f32 v176, v136, s52, v189\n"
    "v_add_f32 v184, v184, v183\n"
    "v_cvt_pk_bf16_f32 v163, v182, v183\n"
    "v_mfma_f32_32x32x16_bf16 v[16:31], a[132:135], v[168:171], v[16:31]\n"
    "v_exp_f32 v176, v176\n"
    "v_fma_f32 v177, v137, s52, v189\n"
    "v_add_f32 v184, v184, v176\n"
    "v_exp_f32 v177, v177\n"
    "v_mfma_f32_32x32x16_bf16 v[32:47], a[136:139], v[168:171], v[32:47]\n"
    "v_fma_f32 v178, v138, s52, v189\n"
    "v_add_f32 v184, v184, v177\n"
    "v_cvt_pk_bf16_f32 v164, v176, v177\n"
    "v_exp_f32 v178, v178\n"
    "v_fma_f32 v179, v139, s52, v189\n"
    "v_mfma_f32_32x32x16_bf16 v[48:63], a[140:143], v[168:171], v[48:63]\n"
    "v_add_f32 v184, v184, v178\n"
    "v_exp_f32 v179, v179\n"
    "v_fma_f32 v180, v140, s52, v189\n"
    "v_add_f32 v184, v184, v179\n"
    "v_cvt_pk_bf16_f32 v165, v178, v179\n"
    "v_mfma_f32_32x32x16_bf16 v[0:15], a[144:147], v[172:175], v[0:15]\n"
    "s_waitcnt lgkmcnt(0)\n"
    "v_rcp_f32 v191, v185\n"
    "v_exp_f32 v180, v180\n"
    "v_fma_f32 v181, v141, s52, v189\n"
    "v_add_f32 v184, v184, v180\n"
    "v_exp_f32 v181, v181\n"
    "v_mfma_f32_32x32x16_bf16 v[16:31], a[148:151], v[172:175], v[16:31]\n"
    "v_fma_f32 v182, v142, s52, v189\n"
    "v_add_f32 v184, v184, v181\n"
    "v_exp_f32 v182, v182\n"
    "v_fma_f32 v183, v143, s52, v189\n"
    "v_add_f32 v184, v184, v182\n"
    "v_mfma_f32_32x32x16_bf16 v[32:47], a[152:155], v[172:175], v[32:47]\n"
    "v_exp_f32 v183, v183\n"
    "v_cvt_pk_bf16_f32 v166, v180, v181\n"
    "v_add_f32 v184, v184, v183\n"
    "v_cvt_pk_bf16_f32 v167, v182, v183\n"
    "v_cmp_nge_f32 vcc, s53, v184\n"
    "v_mfma_f32_32x32x16_bf16 v[48:63], a[156:159], v[172:175], v[48:63]\n"
    "s_cbranch_vccnz rare_stub_70\n"
    "back_70:\n"
    "v_add_f32 v186, v186, v184\n"
    "v_mfma_f32_32x32x16_bf16 v[64:79], a[128:131], v[160:163], v[64:79]\n"
    "s_nop 3\n"
    "v_mul_f32 v224, v0, v191\n"
    "v_mul_f32 v225, v1, v191\n"
    "v_mul_f32 v226, v2, v191\n"
    "v_mul_f32 v227, v3, v191\n"
    "v_mul_f32 v228, v4, v191\n"
    "v_mul_f32 v229, v5, v191\n"
    "v_mul_f32 v230, v6, v191\n"
    "v_mul_f32 v231, v7, v191\n"
    "v_cvt_pk_bf16_f32 v232, v224, v225\n"
    "v_cvt_pk_bf16_f32 v233, v226, v227\n"
    "v_mfma_f32_32x32x16_bf16 v[80:95], a[132:135], v[160:163], v[80:95]\n"
    "v_cvt_pk_bf16_f32 v234, v228, v229\n"
    "v_cvt_pk_bf16_f32 v235, v230, v231\n"
    "ds_write_b128 v202, v[232:235] offset:0\n"
    "v_mul_f32 v224, v8, v191\n"
    "v_mul_f32 v225, v9, v191\n"
    "v_mul_f32 v226, v10, v191\n"
    "v_mul_f32 v227, v11, v191\n"
    "v_mul_f32 v228, v12, v191\n"
    "v_mul_f32 v229, v13, v191\n"
    "v_mul_f32 v230, v14, v191\n"
    "v_mul_f32 v231, v15, v191\n"
    "v_cvt_pk_bf16_f32 v236, v224, v225\n"
    "v_cvt_pk_bf16_f32 v237, v226, v227\n"
    "v_mfma_f32_32x32x16_bf16 v[96:111], a[136:139], v[160:163], v[96:111]\n"
    "v_cvt_pk_bf16_f32 v238, v228, v229\n"
    "v_cvt_pk_bf16_f32 v239, v230, v231\n"
    "ds_write_b128 v203, v[236:239] offset:0\n"
    "v_mul_f32 v224, v16, v191\n"
    "v_mul_f32 v225, v17, v191\n"
    "v_mul_f32 v226, v18, v191\n"
    "v_mul_f32 v227, v19, v191\n"
    "v_mul_f32 v228, v20, v191\n"
    "v_mul_f32 v229, v21, v191\n"
    "v_mul_f32 v230, v22, v191\n"
    "v_mul_f32 v231, v23, v191\n"
    "v_cvt_pk_bf16_f32 v232, v224, v225\n"
    "v_cvt_pk_bf16_f32 v233, v226, v227\n"
    "v_cvt_pk_bf16_f32 v234, v228, v229\n"
    "v_mfma_f32_32x32x16_bf16 v[112:127], a[140:143], v[160:163], v[112:127]\n"
    "v_cvt_pk_bf16_f32 v235, v230, v231\n"
    "ds_write_b128 v204, v[232:235] offset:0\n"
    "v_mul_f32 v224, v24, v191\n"
    "v_mul_f32 v225, v25, v191\n"
    "v_mul_f32 v226, v26, v191\n"
    "v_mul_f32 v227, v27, v191\n"
    "v_mul_f32 v228, v28, v191\n"
    "v_mul_f32 v229, v29, v191\n"
    "v_mul_f32 v230, v30, v191\n"
    "v_mul_f32 v231, v31, v191\n"
    "v_cvt_pk_bf16_f32 v236, v224, v225\n"
    "v_cvt_pk_bf16_f32 v237, v226, v227\n"
    "v_cvt_pk_bf16_f32 v238, v228, v229\n"
    "v_mfma_f32_32x32x16_bf16 v[64:79], a[144:147], v[164:167], v[64:79]\n"
    "v_cvt_pk_bf16_f32 v239, v230, v231\n"
    "ds_write_b128 v205, v[236:239] offset:0\n"
    "v_mul_f32 v224, v32, v191\n"
    "v_mul_f32 v225, v33, v191\n"
    "v_mul_f32 v226, v34, v191\n"
    "v_mul_f32 v227, v35, v191\n"
    "v_mul_f32 v228, v36, v191\n"
    "v_mul_f32 v229, v37, v191\n"
    "v_mul_f32 v230, v38, v191\n"
    "v_mul_f32 v231, v39, v191\n"
    "v_cvt_pk_bf16_f32 v232, v224, v225\n"
    "v_cvt_pk_bf16_f32 v233, v226, v227\n"
    "v_cvt_pk_bf16_f32 v234, v228, v229\n"
    "v_cvt_pk_bf16_f32 v235, v230, v231\n"
    "v_mfma_f32_32x32x16_bf16 v[80:95], a[148:151], v[164:167], v[80:95]\n"
    "ds_write_b128 v206, v[232:235] offset:0\n"
    "v_mul_f32 v224, v40, v191\n"
    "v_mul_f32 v225, v41, v191\n"
    "v_mul_f32 v226, v42, v191\n"
    "v_mul_f32 v227, v43, v191\n"
    "v_mul_f32 v228, v44, v191\n"
    "v_mul_f32 v229, v45, v191\n"
    "v_mul_f32 v230, v46, v191\n"
    "v_mul_f32 v231, v47, v191\n"
    "v_cvt_pk_bf16_f32 v236, v224, v225\n"
    "v_cvt_pk_bf16_f32 v237, v226, v227\n"
    "v_cvt_pk_bf16_f32 v238, v228, v229\n"
    "v_cvt_pk_bf16_f32 v239, v230, v231\n"
    "v_mfma_f32_32x32x16_bf16 v[96:111], a[152:155], v[164:167], v[96:111]\n"
    "ds_write_b128 v207, v[236:239] offset:0\n"
    "v_mul_f32 v224, v48, v191\n"
    "v_mul_f32 v225, v49, v191\n"
    "v_mul_f32 v226, v50, v191\n"
    "v_mul_f32 v227, v51, v191\n"
    "v_mul_f32 v228, v52, v191\n"
    "v_mul_f32 v229, v53, v191\n"
    "v_mul_f32 v230, v54, v191\n"
    "v_mul_f32 v231, v55, v191\n"
    "v_cvt_pk_bf16_f32 v232, v224, v225\n"
    "v_cvt_pk_bf16_f32 v233, v226, v227\n"
    "v_cvt_pk_bf16_f32 v234, v228, v229\n"
    "v_cvt_pk_bf16_f32 v235, v230, v231\n"
    "ds_write_b128 v208, v[232:235] offset:0\n"
    "v_mfma_f32_32x32x16_bf16 v[112:127], a[156:159], v[164:167], v[112:127]\n"
    "v_mul_f32 v224, v56, v191\n"
    "v_mul_f32 v225, v57, v191\n"
    "v_mul_f32 v226, v58, v191\n"
    "v_mul_f32 v227, v59, v191\n"
    "v_mul_f32 v228, v60, v191\n"
    "v_mul_f32 v229, v61, v191\n"
    "v_mul_f32 v230, v62, v191\n"
    "v_mul_f32 v231, v63, v191\n"
    "v_cvt_pk_bf16_f32 v236, v224, v225\n"
    "v_cvt_pk_bf16_f32 v237, v226, v227\n"
    "v_cvt_pk_bf16_f32 v238, v228, v229\n"
    "v_cvt_pk_bf16_f32 v239, v230, v231\n"
    "ds_write_b128 v209, v[236:239] offset:0\n"
    "ds_read_b128 v[0:3], v221 offset:0\n"
    "ds_read_b128 v[4:7], v221 offset:1024\n"
    "ds_read_b128 v[8:11], v221 offset:2048\n"
    "ds_read_b128 v[12:15], v221 offset:3072\n"
    "ds_read_b128 v[16:19], v221 offset:4096\n"
    "ds_read_b128 v[20:23], v221 offset:5120\n"
    "ds_read_b128 v[24:27], v221 offset:6144\n"
    "ds_read_b128 v[28:31], v221 offset:7168\n"
    "v_mov_b32 v252, v186\n"
    "s_nop 1\n"
    "v_permlane32_swap_b32 v252, v186\n"
    "v_add_f32 v186, v186, v252\n"
    "v_rcp_f32 v192, v186\n"
    "v_fma_f32 v176, v144, s52, v190\n"
    "v_exp_f32 v176, v176\n"
    "v_fma_f32 v177, v145, s52, v190\n"
    "v_exp_f32 v177, v177\n"
    "v_fma_f32 v178, v146, s52, v190\n"
    "v_add_f32 v184, v176, v177\n"
    "v_cvt_pk_bf16_f32 v168, v176, v177\n"
    "v_exp_f32 v178, v178\n"
    "v_fma_f32 v179, v147, s52, v190\n"
    "v_add_f32 v184, v184, v178\n"
    "v_exp_f32 v179, v179\n"
    "v_fma_f32 v180, v148, s52, v190\n"
    "s_mov_b32 s55, 25165824\n"
    "s_waitcnt lgkmcnt(7)\n"
    "buffer_store_dwordx4 v[0:3], v214, s[48:51], s55 offen nt\n"
    "v_add_f32 v184, v184, v179\n"
    "v_cvt_pk_bf16_f32 v169, v178, v179\n"
    "v_exp_f32 v180, v180\n"
    "v_fma_f32 v181, v149, s52, v190\n"
    "v_add_f32 v184, v184, v180\n"
    "v_exp_f32 v181, v181\n"
    "v_fma_f32 v182, v150, s52, v190\n"
    "v_add_f32 v184, v184, v181\n"
    "v_cvt_pk_bf16_f32 v170, v180, v181\n"
    "v_exp_f32 v182, v182\n"
    "v_fma_f32 v183, v151, s52, v190\n"
    "v_add_f32 v184, v184, v182\n"
    "v_exp_f32 v183, v183\n"
    "v_fma_f32 v176, v152, s52, v190\n"
    "v_add_f32 v184, v184, v183\n"
    "s_waitcnt lgkmcnt(0)\n"
    "buffer_store_dwordx4 v[4:7], v215, s[48:51], s55 offen offset:1024 nt\n"
    "v_cvt_pk_bf16_f32 v171, v182, v183\n"
    "v_exp_f32 v176, v176\n"
    "v_fma_f32 v177, v153, s52, v190\n"
    "v_add_f32 v184, v184, v176\n"
    "v_exp_f32 v177, v177\n"
    "v_fma_f32 v178, v154, s52, v190\n"
    "v_add_f32 v184, v184, v177\n"
    "v_cvt_pk_bf16_f32 v172, v176, v177\n"
    "v_exp_f32 v178, v178\n"
    "v_fma_f32 v179, v155, s52, v190\n"
    "v_add_f32 v184, v184, v178\n"
    "v_exp_f32 v179, v179\n"
    "v_fma_f32 v180, v156, s52, v190\n"
    "v_add_f32 v184, v184, v179\n"
    "v_cvt_pk_bf16_f32 v173, v178, v179\n"
    "v_exp_f32 v180, v180\n"
    "buffer_store_dwordx4 v[8:11], v216, s[48:51], s55 offen offset:2048 nt\n"
    "v_fma_f32 v181, v157, s52, v190\n"
    "v_add_f32 v184, v184, v180\n"
    "v_exp_f32 v181, v181\n"
    "v_fma_f32 v182, v158, s52, v190\n"
    "v_add_f32 v184, v184, v181\n"
    "v_exp_f32 v182, v182\n"
    "v_fma_f32 v183, v159, s52, v190\n"
    "v_add_f32 v184, v184, v182\n"
    "v_exp_f32 v183, v183\n"
    "v_cvt_pk_bf16_f32 v174, v180, v181\n"
    "v_add_f32 v184, v184, v183\n"
    "v_cvt_pk_bf16_f32 v175, v182, v183\n"
    "v_cmp_nge_f32 vcc, s53, v184\n"
    "s_cbranch_vccnz rare_stub_71\n"
    "back_71:\n"
    "v_add_f32 v187, v187, v184\n"
    "buffer_store_dwordx4 v[12:15], v217, s[48:51], s55 offen offset:3072 nt\n"
    "v_mfma_f32_32x32x16_bf16 a[160:175], a[128:131], v[168:171], a[160:175]\n"
    "v_mov_b32 v252, v187\n"
    "s_nop 1\n"
    "v_permlane32_swap_b32 v252, v187\n"
    "v_add_f32 v187, v187, v252\n"
    "v_rcp_f32 v193, v187\n"
    "v_mul_f32 v224, v64, v192\n"
    "v_mul_f32 v225, v65, v192\n"
    "v_mul_f32 v226, v66, v192\n"
    "v_mul_f32 v227, v67, v192\n"
    "v_mul_f32 v228, v68, v192\n"
    "v_mul_f32 v229, v69, v192\n"
    "v_mul_f32 v230, v70, v192\n"
    "v_mul_f32 v231, v71, v192\n"
    "v_cvt_pk_bf16_f32 v232, v224, v225\n"
    "v_mfma_f32_32x32x16_bf16 a[176:191], a[132:135], v[168:171], a[176:191]\n"
    "v_cvt_pk_bf16_f32 v233, v226, v227\n"
    "v_cvt_pk_bf16_f32 v234, v228, v229\n"
    "v_cvt_pk_bf16_f32 v235, v230, v231\n"
    "ds_write_b128 v202, v[232:235] offset:8192\n"
    "v_mul_f32 v224, v72, v192\n"
    "v_mul_f32 v225, v73, v192\n"
    "v_mul_f32 v226, v74, v192\n"
    "v_mul_f32 v227, v75, v192\n"
    "v_mul_f32 v228, v76, v192\n"
    "v_mul_f32 v229, v77, v192\n"
    "v_mul_f32 v230, v78, v192\n"
    "v_mul_f32 v231, v79, v192\n"
    "v_cvt_pk_bf16_f32 v236, v224, v225\n"
    "v_cvt_pk_bf16_f32 v237, v226, v227\n"
    "s_mov_b32 s55, 25169920\n"
    "v_mfma_f32_32x32x16_bf16 a[192:207], a[136:139], v[168:171], a[192:207]\n"
    "buffer_store_dwordx4 v[16:19], v214, s[48:51], s55 offen nt\n"
    "v_cvt_pk_bf16_f32 v238, v228, v229\n"
    "v_cvt_pk_bf16_f32 v239, v230, v231\n"
    "ds_write_b128 v203, v[236:239] offset:8192\n"
    "v_mul_f32 v224, v80, v192\n"
    "v_mul_f32 v225, v81, v192\n"
    "v_mul_f32 v226, v82, v192\n"
    "v_mul_f32 v227, v83, v192\n"
    "v_mul_f32 v228, v84, v192\n"
    "v_mul_f32 v229, v85, v192\n"
    "v_mul_f32 v230, v86, v192\n"
    "v_mul_f32 v231, v87, v192\n"
    "v_cvt_pk_bf16_f32 v232, v224, v225\n"
    "v_cvt_pk_bf16_f32 v233, v226, v227\n"
    "v_mfma_f32_32x32x16_bf16 a[208:223], a[140:143], v[168:171], a[208:223]\n"
    "v_cvt_pk_bf16_f32 v234, v228, v229\n"
    "v_cvt_pk_bf16_f32 v235, v230, v231\n"
    "ds_write_b128 v204, v[232:235] offset:8192\n"
    "v_mul_f32 v224, v88, v192\n"
    "v_mul_f32 v225, v89, v192\n"
    "v_mul_f32 v226, v90, v192\n"
    "v_mul_f32 v227, v91, v192\n"
    "v_mul_f32 v228, v92, v192\n"
    "v_mul_f32 v229, v93, v192\n"
    "v_mul_f32 v230, v94, v192\n"
    "v_mul_f32 v231, v95, v192\n"
    "v_cvt_pk_bf16_f32 v236, v224, v225\n"
    "v_cvt_pk_bf16_f32 v237, v226, v227\n"
    "v_cvt_pk_bf16_f32 v238, v228, v229\n"
    "v_mfma_f32_32x32x16_bf16 a[160:175], a[144:147], v[172:175], a[160:175]\n"
    "buffer_store_dwordx4 v[20:23], v215, s[48:51], s55 offen offset:1024 nt\n"
    "v_cvt_pk_bf16_f32 v239, v230, v231\n"
    "ds_write_b128 v205, v[236:239] offset:8192\n"
    "v_mul_f32 v224, v96, v192\n"
    "v_mul_f32 v225, v97, v192\n"
    "v_mul_f32 v226, v98, v192\n"
    "v_mul_f32 v227, v99, v192\n"
    "v_mul_f32 v228, v100, v192\n"
    "v_mul_f32 v229, v101, v192\n"
    "v_mul_f32 v230, v102, v192\n"
    "v_mul_f32 v231, v103, v192\n"
    "v_cvt_pk_bf16_f32 v232, v224, v225\n"
    "v_cvt_pk_bf16_f32 v233, v226, v227\n"
    "v_cvt_pk_bf16_f32 v234, v228, v229\n"
    "v_mfma_f32_32x32x16_bf16 a[176:191], a[148:151], v[172:175], a[176:191]\n"
    "v_cvt_pk_bf16_f32 v235, v230, v231\n"
    "ds_write_b128 v206, v[232:235] offset:8192\n"
    "v_mul_f32 v224, v104, v192\n"
    "v_mul_f32 v225, v105, v192\n"
    "v_mul_f32 v226, v106, v192\n"
    "v_mul_f32 v227, v107, v192\n"
    "v_mul_f32 v228, v108, v192\n"
    "v_mul_f32 v229, v109, v192\n"
    "v_mul_f32 v230, v110, v192\n"
    "v_mul_f32 v231, v111, v192\n"
    "v_cvt_pk_bf16_f32 v236, v224, v225\n"
    "v_cvt_pk_bf16_f32 v237, v226, v227\n"
    "v_cvt_pk_bf16_f32 v238, v228, v229\n"
    "v_cvt_pk_bf16_f32 v239, v230, v231\n"
    "buffer_store_dwordx4 v[24:27], v216, s[48:51], s55 offen offset:2048 nt\n"
    "v_mfma_f32_32x32x16_bf16 a[192:207], a[152:155], v[172:175], a[192:207]\n"
    "ds_write_b128 v207, v[236:239] offset:8192\n"
    "v_mul_f32 v224, v112, v192\n"
    "v_mul_f32 v225, v113, v192\n"
    "v_mul_f32 v226, v114, v192\n"
    "v_mul_f32 v227, v115, v192\n"
    "v_mul_f32 v228, v116, v192\n"
    "v_mul_f32 v229, v117, v192\n"
    "v_mul_f32 v230, v118, v192\n"
    "v_mul_f32 v231, v119, v192\n"
    "v_cvt_pk_bf16_f32 v232, v224, v225\n"
    "v_cvt_pk_bf16_f32 v233, v226, v227\n"
    "v_cvt_pk_bf16_f32 v234, v228, v229\n"
    "v_cvt_pk_bf16_f32 v235, v230, v231\n"
    "ds_write_b128 v208, v[232:235] offset:8192\n"
    "v_mfma_f32_32x32x16_bf16 a[208:223], a[156:159], v[172:175], a[208:223]\n"
    "v_mul_f32 v224, v120, v192\n"
    "v_mul_f32 v225, v121, v192\n"
    "v_mul_f32 v226, v122, v192\n"
    "v_mul_f32 v227, v123, v192\n"
    "v_mul_f32 v228, v124, v192\n"
    "v_mul_f32 v229, v125, v192\n"
    "v_mul_f32 v230, v126, v192\n"
    "v_mul_f32 v231, v127, v192\n"
    "v_cvt_pk_bf16_f32 v236, v224, v225\n"
    "v_cvt_pk_bf16_f32 v237, v226, v227\n"
    "v_cvt_pk_bf16_f32 v238, v228, v229\n"
    "v_cvt_pk_bf16_f32 v239, v230, v231\n"
    "ds_write_b128 v209, v[236:239] offset:8192\n"
    "buffer_store_dwordx4 v[28:31], v217, s[48:51], s55 offen offset:3072 nt\n"
    "ds_read_b128 v[64:67], v221 offset:8192\n"
    "ds_read_b128 v[68:71], v221 offset:9216\n"
    "ds_read_b128 v[72:75], v221 offset:10240\n"
    "ds_read_b128 v[76:79], v221 offset:11264\n"
    "ds_read_b128 v[80:83], v221 offset:12288\n"
    "ds_read_b128 v[84:87], v221 offset:13312\n"
    "ds_read_b128 v[88:91], v221 offset:14336\n"
    "ds_read_b128 v[92:95], v221 offset:15360\n"
    "v_accvgpr_read_b32 v224, a160\n"
    "v_accvgpr_read_b32 v225, a161\n"
    "v_accvgpr_read_b32 v226, a162\n"
    "v_accvgpr_read_b32 v227, a163\n"
    "v_accvgpr_read_b32 v228, a164\n"
    "v_accvgpr_read_b32 v229, a165\n"
    "v_accvgpr_read_b32 v230, a166\n"
    "v_accvgpr_read_b32 v231, a167\n"
    "v_mul_f32 v224, v224, v193\n"
    "v_mul_f32 v225, v225, v193\n"
    "v_mul_f32 v226, v226, v193\n"
    "v_mul_f32 v227, v227, v193\n"
    "v_mul_f32 v228, v228, v193\n"
    "v_mul_f32 v229, v229, v193\n"
    "v_mul_f32 v230, v230, v193\n"
    "v_mul_f32 v231, v231, v193\n"
    "v_cvt_pk_bf16_f32 v232, v224, v225\n"
    "v_cvt_pk_bf16_f32 v233, v226, v227\n"
    "v_cvt_pk_bf16_f32 v234, v228, v229\n"
    "v_cvt_pk_bf16_f32 v235, v230, v231\n"
    "ds_write_b128 v202, v[232:235] offset:16384\n"
    "s_mov_b32 s55, 25174016\n"
    "s_waitcnt lgkmcnt(8)\n"
    "buffer_store_dwordx4 v[64:67], v214, s[48:51], s55 offen nt\n"
    "v_accvgpr_read_b32 v224, a168\n"
    "v_accvgpr_read_b32 v225, a169\n"
    "v_accvgpr_read_b32 v226, a170\n"
    "v_accvgpr_read_b32 v227, a171\n"
    "v_accvgpr_read_b32 v228, a172\n"
    "v_accvgpr_read_b32 v229, a173\n"
    "v_accvgpr_read_b32 v230, a174\n"
    "v_accvgpr_read_b32 v231, a175\n"
    "v_mul_f32 v224, v224, v193\n"
    "v_mul_f32 v225, v225, v193\n"
    "v_mul_f32 v226, v226, v193\n"
    "v_mul_f32 v227, v227, v193\n"
    "v_mul_f32 v228, v228, v193\n"
    "v_mul_f32 v229, v229, v193\n"
    "v_mul_f32 v230, v230, v193\n"
    "v_mul_f32 v231, v231, v193\n"
    "v_cvt_pk_bf16_f32 v236, v224, v225\n"
    "v_cvt_pk_bf16_f32 v237, v226, v227\n"
    "v_cvt_pk_bf16_f32 v238, v228, v229\n"
    "v_cvt_pk_bf16_f32 v239, v230, v231\n"
    "ds_write_b128 v203, v[236:239] offset:16384\n"
    "s_waitcnt lgkmcnt(2)\n"
    "buffer_store_dwordx4 v[68:71], v215, s[48:51], s55 offen offset:1024 nt\n"
    "v_accvgpr_read_b32 v224, a176\n"
    "v_accvgpr_read_b32 v225, a177\n"
    "v_accvgpr_read_b32 v226, a178\n"
    "v_accvgpr_read_b32 v227, a179\n"
    "v_accvgpr_read_b32 v228, a180\n"
    "v_accvgpr_read_b32 v229, a181\n"
    "v_accvgpr_read_b32 v230, a182\n"
    "v_accvgpr_read_b32 v231, a183\n"
    "v_mul_f32 v224, v224, v193\n"
    "v_mul_f32 v225, v225, v193\n"
    "v_mul_f32 v226, v226, v193\n"
    "v_mul_f32 v227, v227, v193\n"
    "v_mul_f32 v228, v228, v193\n"
    "v_mul_f32 v229, v229, v193\n"
    "v_mul_f32 v230, v230, v193\n"
    "v_mul_f32 v231, v231, v193\n"
    "v_cvt_pk_bf16_f32 v232, v224, v225\n"
    "v_cvt_pk_bf16_f32 v233, v226, v227\n"
    "v_cvt_pk_bf16_f32 v234, v228, v229\n"
    "v_cvt_pk_bf16_f32 v235, v230, v231\n"
    "ds_write_b128 v204, v[232:235] offset:16384\n"
    "buffer_store_dwordx4 v[72:75], v216, s[48:51], s55 offen offset:2048 nt\n"
    "v_accvgpr_read_b32 v224, a184\n"
    "v_accvgpr_read_b32 v225, a185\n"
    "v_accvgpr_read_b32 v226, a186\n"
    "v_accvgpr_read_b32 v227, a187\n"
    "v_accvgpr_read_b32 v228, a188\n"
    "v_accvgpr_read_b32 v229, a189\n"
    "v_accvgpr_read_b32 v230, a190\n"
    "v_accvgpr_read_b32 v231, a191\n"
    "v_mul_f32 v224, v224, v193\n"
    "v_mul_f32 v225, v225, v193\n"
    "v_mul_f32 v226, v226, v193\n"
    "v_mul_f32 v227, v227, v193\n"
    "v_mul_f32 v228, v228, v193\n"
    "v_mul_f32 v229, v229, v193\n"
    "v_mul_f32 v230, v230, v193\n"
    "v_mul_f32 v231, v231, v193\n"
    "v_cvt_pk_bf16_f32 v236, v224, v225\n"
    "v_cvt_pk_bf16_f32 v237, v226, v227\n"
    "v_cvt_pk_bf16_f32 v238, v228, v229\n"
    "v_cvt_pk_bf16_f32 v239, v230, v231\n"
    "ds_write_b128 v205, v[236:239] offset:16384\n"
    "buffer_store_dwordx4 v[76:79], v217, s[48:51], s55 offen offset:3072 nt\n"
    "v_accvgpr_read_b32 v224, a192\n"
    "v_accvgpr_read_b32 v225, a193\n"
    "v_accvgpr_read_b32 v226, a194\n"
    "v_accvgpr_read_b32 v227, a195\n"
    "v_accvgpr_read_b32 v228, a196\n"
    "v_accvgpr_read_b32 v229, a197\n"
    "v_accvgpr_read_b32 v230, a198\n"
    "v_accvgpr_read_b32 v231, a199\n"
    "v_mul_f32 v224, v224, v193\n"
    "v_mul_f32 v225, v225, v193\n"
    "v_mul_f32 v226, v226, v193\n"
    "v_mul_f32 v227, v227, v193\n"
    "v_mul_f32 v228, v228, v193\n"
    "v_mul_f32 v229, v229, v193\n"
    "v_mul_f32 v230, v230, v193\n"
    "v_mul_f32 v231, v231, v193\n"
    "v_cvt_pk_bf16_f32 v232, v224, v225\n"
    "v_cvt_pk_bf16_f32 v233, v226, v227\n"
    "v_cvt_pk_bf16_f32 v234, v228, v229\n"
    "v_cvt_pk_bf16_f32 v235, v230, v231\n"
    "ds_write_b128 v206, v[232:235] offset:16384\n"
    "s_mov_b32 s55, 25178112\n"
    "buffer_store_dwordx4 v[80:83], v214, s[48:51], s55 offen nt\n"
    "v_accvgpr_read_b32 v224, a200\n"
    "v_accvgpr_read_b32 v225, a201\n"
    "v_accvgpr_read_b32 v226, a202\n"
    "v_accvgpr_read_b32 v227, a203\n"
    "v_accvgpr_read_b32 v228, a204\n"
    "v_accvgpr_read_b32 v229, a205\n"
    "v_accvgpr_read_b32 v230, a206\n"
    "v_accvgpr_read_b32 v231, a207\n"
    "v_mul_f32 v224, v224, v193\n"
    "v_mul_f32 v225, v225, v193\n"
    "v_mul_f32 v226, v226, v193\n"
    "v_mul_f32 v227, v227, v193\n"
    "v_mul_f32 v228, v228, v193\n"
    "v_mul_f32 v229, v229, v193\n"
    "v_mul_f32 v230, v230, v193\n"
    "v_mul_f32 v231, v231, v193\n"
    "v_cvt_pk_bf16_f32 v236, v224, v225\n"
    "v_cvt_pk_bf16_f32 v237, v226, v227\n"
    "v_cvt_pk_bf16_f32 v238, v228, v229\n"
    "v_cvt_pk_bf16_f32 v239, v230, v231\n"
    "ds_write_b128 v207, v[236:239] offset:16384\n"
    "buffer_store_dwordx4 v[84:87], v215, s[48:51], s55 offen offset:1024 nt\n"
    "v_accvgpr_read_b32 v224, a208\n"
    "v_accvgpr_read_b32 v225, a209\n"
    "v_accvgpr_read_b32 v226, a210\n"
    "v_accvgpr_read_b32 v227, a211\n"
    "v_accvgpr_read_b32 v228, a212\n"
    "v_accvgpr_read_b32 v229, a213\n"
    "v_accvgpr_read_b32 v230, a214\n"
    "v_accvgpr_read_b32 v231, a215\n"
    "v_mul_f32 v224, v224, v193\n"
    "v_mul_f32 v225, v225, v193\n"
    "v_mul_f32 v226, v226, v193\n"
    "v_mul_f32 v227, v227, v193\n"
    "v_mul_f32 v228, v228, v193\n"
    "v_mul_f32 v229, v229, v193\n"
    "v_mul_f32 v230, v230, v193\n"
    "v_mul_f32 v231, v231, v193\n"
    "v_cvt_pk_bf16_f32 v232, v224, v225\n"
    "v_cvt_pk_bf16_f32 v233, v226, v227\n"
    "v_cvt_pk_bf16_f32 v234, v228, v229\n"
    "v_cvt_pk_bf16_f32 v235, v230, v231\n"
    "ds_write_b128 v208, v[232:235] offset:16384\n"
    "buffer_store_dwordx4 v[88:91], v216, s[48:51], s55 offen offset:2048 nt\n"
    "v_accvgpr_read_b32 v224, a216\n"
    "v_accvgpr_read_b32 v225, a217\n"
    "v_accvgpr_read_b32 v226, a218\n"
    "v_accvgpr_read_b32 v227, a219\n"
    "v_accvgpr_read_b32 v228, a220\n"
    "v_accvgpr_read_b32 v229, a221\n"
    "v_accvgpr_read_b32 v230, a222\n"
    "v_accvgpr_read_b32 v231, a223\n"
    "v_mul_f32 v224, v224, v193\n"
    "v_mul_f32 v225, v225, v193\n"
    "v_mul_f32 v226, v226, v193\n"
    "v_mul_f32 v227, v227, v193\n"
    "v_mul_f32 v228, v228, v193\n"
    "v_mul_f32 v229, v229, v193\n"
    "v_mul_f32 v230, v230, v193\n"
    "v_mul_f32 v231, v231, v193\n"
    "v_cvt_pk_bf16_f32 v236, v224, v225\n"
    "v_cvt_pk_bf16_f32 v237, v226, v227\n"
    "v_cvt_pk_bf16_f32 v238, v228, v229\n"
    "v_cvt_pk_bf16_f32 v239, v230, v231\n"
    "ds_write_b128 v209, v[236:239] offset:16384\n"
    "buffer_store_dwordx4 v[92:95], v217, s[48:51], s55 offen offset:3072 nt\n"
    "ds_read_b128 v[32:35], v221 offset:16384\n"
    "ds_read_b128 v[36:39], v221 offset:17408\n"
    "ds_read_b128 v[40:43], v221 offset:18432\n"
    "ds_read_b128 v[44:47], v221 offset:19456\n"
    "ds_read_b128 v[48:51], v221 offset:20480\n"
    "ds_read_b128 v[52:55], v221 offset:21504\n"
    "ds_read_b128 v[56:59], v221 offset:22528\n"
    "ds_read_b128 v[60:63], v221 offset:23552\n"
    "s_mov_b32 s55, 25182208\n"
    "s_waitcnt lgkmcnt(7)\n"
    "buffer_store_dwordx4 v[32:35], v214, s[48:51], s55 offen nt\n"
    "s_waitcnt lgkmcnt(6)\n"
    "buffer_store_dwordx4 v[36:39], v215, s[48:51], s55 offen offset:1024 nt\n"
    "s_waitcnt lgkmcnt(5)\n"
    "buffer_store_dwordx4 v[40:43], v216, s[48:51], s55 offen offset:2048 nt\n"
    "s_waitcnt lgkmcnt(4)\n"
    "buffer_store_dwordx4 v[44:47], v217, s[48:51], s55 offen offset:3072 nt\n"
    "s_mov_b32 s55, 25186304\n"
    "s_waitcnt lgkmcnt(3)\n"
    "buffer_store_dwordx4 v[48:51], v214, s[48:51], s55 offen nt\n"
    "s_waitcnt lgkmcnt(2)\n"
    "buffer_store_dwordx4 v[52:55], v215, s[48:51], s55 offen offset:1024 nt\n"
    "s_waitcnt lgkmcnt(1)\n"
    "buffer_store_dwordx4 v[56:59], v216, s[48:51], s55 offen offset:2048 nt\n"
    "s_waitcnt lgkmcnt(0)\n"
    "buffer_store_dwordx4 v[60:63], v217, s[48:51], s55 offen offset:3072 nt\n"
    "s_endpgm\n"
    "code_end:\n"
    "rare_stub_3:\n"
    "s_call_b64 s[60:61], rare_0_1\n"
    "s_branch back_3\n"
    "rare_stub_4:\n"
    "s_call_b64 s[60:61], rare_1_0\n"
    "s_branch back_4\n"
    "rare_stub_5:\n"
    "s_call_b64 s[60:61], rare_2_1\n"
    "s_branch back_5\n"
    "rare_stub_6:\n"
    "s_call_b64 s[60:61], rare_0_0\n"
    "s_branch back_6\n"
    "rare_stub_7:\n"
    "s_call_b64 s[60:61], rare_1_1\n"
    "s_branch back_7\n"
    "rare_stub_8:\n"
    "s_call_b64 s[60:61], rare_2_0\n"
    "s_branch back_8\n"
    "rare_stub_9:\n"
    "s_call_b64 s[60:61], rare_0_1\n"
    "s_branch back_9\n"
    "rare_stub_34:\n"
    "s_call_b64 s[60:61], rare_1_0\n"
    "s_branch back_34\n"
    "rare_stub_35:\n"
    "s_call_b64 s[60:61], rare_2_1\n"
    "s_branch back_35\n"
    "rare_stub_39:\n"
    "s_call_b64 s[60:61], rare_0_1\n"
    "s_branch back_39\n"
    "rare_stub_40:\n"
    "s_call_b64 s[60:61], rare_1_0\n"
    "s_branch back_40\n"
    "rare_stub_41:\n"
    "s_call_b64 s[60:61], rare_2_1\n"
    "s_branch back_41\n"
    "rare_stub_42:\n"
    "s_call_b64 s[60:61], rare_0_0\n"
    "s_branch back_42\n"
    "rare_stub_43:\n"
    "s_call_b64 s[60:61], rare_1_1\n"
    "s_branch back_43\n"
    "rare_stub_44:\n"
    "s_call_b64 s[60:61], rare_2_0\n"
    "s_branch back_44\n"
    "rare_stub_45:\n"
    "s_call_b64 s[60:61], rare_0_1\n"
    "s_branch back_45\n"
    "rare_stub_70:\n"
    "s_call_b64 s[60:61], rare_1_0\n"
    "s_branch back_70\n"
    "rare_stub_71:\n"
    "s_call_b64 s[60:61], rare_2_1\n"
    "s_branch back_71\n"
    "rare_0_0:\n"
    "s_nop 7\n"
    "s_nop 7\n"
    "v_max3_f32 v248, v128, v129, v130\n"
    "v_max3_f32 v248, v248, v131, v132\n"
    "v_max3_f32 v248, v248, v133, v134\n"
    "v_max3_f32 v248, v248, v135, v136\n"
    "v_max3_f32 v248, v248, v137, v138\n"
    "v_max3_f32 v248, v248, v139, v140\n"
    "v_max3_f32 v248, v248, v141, v142\n"
    "v_max_f32 v248, v248, v143\n"
    "v_mov_b32 v249, v248\n"
    "s_nop 1\n"
    "v_permlane32_swap_b32 v249, v248\n"
    "v_max_f32 v248, v248, v249\n"
    "v_mul_f32_e64 v248, -s52, v248\n"
    "v_min_f32 v248, v248, v188\n"
    "v_sub_f32 v249, v248, v188\n"
    "v_exp_f32 v249, v249\n"
    "v_mov_b32 v188, v248\n"
    "v_mul_f32 v185, v185, v249\n"
    "v_mul_f32 v0, v0, v249\n"
    "v_mul_f32 v1, v1, v249\n"
    "v_mul_f32 v2, v2, v249\n"
    "v_mul_f32 v3, v3, v249\n"
    "v_mul_f32 v4, v4, v249\n"
    "v_mul_f32 v5, v5, v249\n"
    "v_mul_f32 v6, v6, v249\n"
    "v_mul_f32 v7, v7, v249\n"
    "v_mul_f32 v8, v8, v249\n"
    "v_mul_f32 v9, v9, v249\n"
    "v_mul_f32 v10, v10, v249\n"
    "v_mul_f32 v11, v11, v249\n"
    "v_mul_f32 v12, v12, v249\n"
    "v_mul_f32 v13, v13, v249\n"
    "v_mul_f32 v14, v14, v249\n"
    "v_mul_f32 v15, v15, v249\n"
    "v_mul_f32 v16, v16, v249\n"
    "v_mul_f32 v17, v17, v249\n"
    "v_mul_f32 v18, v18, v249\n"
    "v_mul_f32 v19, v19, v249\n"
    "v_mul_f32 v20, v20, v249\n"
    "v_mul_f32 v21, v21, v249\n"
    "v_mul_f32 v22, v22, v249\n"
    "v_mul_f32 v23, v23, v249\n"
    "v_mul_f32 v24, v24, v249\n"
    "v_mul_f32 v25, v25, v249\n"
    "v_mul_f32 v26, v26, v249\n"
    "v_mul_f32 v27, v27, v249\n"
    "v_mul_f32 v28, v28, v249\n"
    "v_mul_f32 v29, v29, v249\n"
    "v_mul_f32 v30, v30, v249\n"
    "v_mul_f32 v31, v31, v249\n"
    "v_mul_f32 v32, v32, v249\n"
    "v_mul_f32 v33, v33, v249\n"
    "v_mul_f32 v34, v34, v249\n"
    "v_mul_f32 v35, v35, v249\n"
    "v_mul_f32 v36, v36, v249\n"
    "v_mul_f32 v37, v37, v249\n"
    "v_mul_f32 v38, v38, v249\n"
    "v_mul_f32 v39, v39, v249\n"
    "v_mul_f32 v40, v40, v249\n"
    "v_mul_f32 v41, v41, v249\n"
    "v_mul_f32 v42, v42, v249\n"
    "v_mul_f32 v43, v43, v249\n"
    "v_mul_f32 v44, v44, v249\n"
    "v_mul_f32 v45, v45, v249\n"
    "v_mul_f32 v46, v46, v249\n"
    "v_mul_f32 v47, v47, v249\n"
    "v_mul_f32 v48, v48, v249\n"
    "v_mul_f32 v49, v49, v249\n"
    "v_mul_f32 v50, v50, v249\n"
    "v_mul_f32 v51, v51, v249\n"
    "v_mul_f32 v52, v52, v249\n"
    "v_mul_f32 v53, v53, v249\n"
    "v_mul_f32 v54, v54, v249\n"
    "v_mul_f32 v55, v55, v249\n"
    "v_mul_f32 v56, v56, v249\n"
    "v_mul_f32 v57, v57, v249\n"
    "v_mul_f32 v58, v58, v249\n"
    "v_mul_f32 v59, v59, v249\n"
    "v_mul_f32 v60, v60, v249\n"
    "v_mul_f32 v61, v61, v249\n"
    "v_mul_f32 v62, v62, v249\n"
    "v_mul_f32 v63, v63, v249\n"
    "v_fma_f32 v176, v128, s52, v188\n"
    "v_exp_f32 v176, v176\n"
    "v_fma_f32 v177, v129, s52, v188\n"
    "v_exp_f32 v177, v177\n"
    "v_fma_f32 v178, v130, s52, v188\n"
    "v_add_f32 v184, v176, v177\n"
    "v_cvt_pk_bf16_f32 v160, v176, v177\n"
    "v_exp_f32 v178, v178\n"
    "v_fma_f32 v179, v131, s52, v188\n"
    "v_add_f32 v184, v184, v178\n"
    "v_exp_f32 v179, v179\n"
    "v_fma_f32 v180, v132, s52, v188\n"
    "v_add_f32 v184, v184, v179\n"
    "v_cvt_pk_bf16_f32 v161, v178, v179\n"
    "v_exp_f32 v180, v180\n"
    "v_fma_f32 v181, v133, s52, v188\n"
    "v_add_f32 v184, v184, v180\n"
    "v_exp_f32 v181, v181\n"
    "v_fma_f32 v182, v134, s52, v188\n"
    "v_add_f32 v184, v184, v181\n"
    "v_cvt_pk_bf16_f32 v162, v180, v181\n"
    "v_exp_f32 v182, v182\n"
    "v_fma_f32 v183, v135, s52, v188\n"
    "v_add_f32 v184, v184, v182\n"
    "v_exp_f32 v183, v183\n"
    "v_fma_f32 v176, v136, s52, v188\n"
    "v_add_f32 v184, v184, v183\n"
    "v_cvt_pk_bf16_f32 v163, v182, v183\n"
    "v_exp_f32 v176, v176\n"
    "v_fma_f32 v177, v137, s52, v188\n"
    "v_add_f32 v184, v184, v176\n"
    "v_exp_f32 v177, v177\n"
    "v_fma_f32 v178, v138, s52, v188\n"
    "v_add_f32 v184, v184, v177\n"
    "v_cvt_pk_bf16_f32 v164, v176, v177\n"
    "v_exp_f32 v178, v178\n"
    "v_fma_f32 v179, v139, s52, v188\n"
    "v_add_f32 v184, v184, v178\n"
    "v_exp_f32 v179, v179\n"
    "v_fma_f32 v180, v140, s52, v188\n"
    "v_add_f32 v184, v184, v179\n"
    "v_cvt_pk_bf16_f32 v165, v178, v179\n"
    "v_exp_f32 v180, v180\n"
    "v_fma_f32 v181, v141, s52, v188\n"
    "v_add_f32 v184, v184, v180\n"
    "v_exp_f32 v181, v181\n"
    "v_fma_f32 v182, v142, s52, v188\n"
    "v_add_f32 v184, v184, v181\n"
    "v_exp_f32 v182, v182\n"
    "v_fma_f32 v183, v143, s52, v188\n"
    "v_add_f32 v184, v184, v182\n"
    "v_exp_f32 v183, v183\n"
    "v_cvt_pk_bf16_f32 v166, v180, v181\n"
    "v_add_f32 v184, v184, v183\n"
    "v_cvt_pk_bf16_f32 v167, v182, v183\n"
    "s_nop 3\n"
    "s_setpc_b64 s[60:61]\n"
    "rare_0_1:\n"
    "s_nop 7\n"
    "s_nop 7\n"
    "v_max3_f32 v248, v144, v145, v146\n"
    "v_max3_f32 v248, v248, v147, v148\n"
    "v_max3_f32 v248, v248, v149, v150\n"
    "v_max3_f32 v248, v248, v151, v152\n"
    "v_max3_f32 v248, v248, v153, v154\n"
    "v_max3_f32 v248, v248, v155, v156\n"
    "v_max3_f32 v248, v248, v157, v158\n"
    "v_max_f32 v248, v248, v159\n"
    "v_mov_b32 v249, v248\n"
    "s_nop 1\n"
    "v_permlane32_swap_b32 v249, v248\n"
    "v_max_f32 v248, v248, v249\n"
    "v_mul_f32_e64 v248, -s52, v248\n"
    "v_min_f32 v248, v248, v188\n"
    "v_sub_f32 v249, v248, v188\n"
    "v_exp_f32 v249, v249\n"
    "v_mov_b32 v188, v248\n"
    "v_mul_f32 v185, v185, v249\n"
    "v_mul_f32 v0, v0, v249\n"
    "v_mul_f32 v1, v1, v249\n"
    "v_mul_f32 v2, v2, v249\n"
    "v_mul_f32 v3, v3, v249\n"
    "v_mul_f32 v4, v4, v249\n"
    "v_mul_f32 v5, v5, v249\n"
    "v_mul_f32 v6, v6, v249\n"
    "v_mul_f32 v7, v7, v249\n"
    "v_mul_f32 v8, v8, v249\n"
    "v_mul_f32 v9, v9, v249\n"
    "v_mul_f32 v10, v10, v249\n"
    "v_mul_f32 v11, v11, v249\n"
    "v_mul_f32 v12, v12, v249\n"
    "v_mul_f32 v13, v13, v249\n"
    "v_mul_f32 v14, v14, v249\n"
    "v_mul_f32 v15, v15, v249\n"
    "v_mul_f32 v16, v16, v249\n"
    "v_mul_f32 v17, v17, v249\n"
    "v_mul_f32 v18, v18, v249\n"
    "v_mul_f32 v19, v19, v249\n"
    "v_mul_f32 v20, v20, v249\n"
    "v_mul_f32 v21, v21, v249\n"
    "v_mul_f32 v22, v22, v249\n"
    "v_mul_f32 v23, v23, v249\n"
    "v_mul_f32 v24, v24, v249\n"
    "v_mul_f32 v25, v25, v249\n"
    "v_mul_f32 v26, v26, v249\n"
    "v_mul_f32 v27, v27, v249\n"
    "v_mul_f32 v28, v28, v249\n"
    "v_mul_f32 v29, v29, v249\n"
    "v_mul_f32 v30, v30, v249\n"
    "v_mul_f32 v31, v31, v249\n"
    "v_mul_f32 v32, v32, v249\n"
    "v_mul_f32 v33, v33, v249\n"
    "v_mul_f32 v34, v34, v249\n"
    "v_mul_f32 v35, v35, v249\n"
    "v_mul_f32 v36, v36, v249\n"
    "v_mul_f32 v37, v37, v249\n"
    "v_mul_f32 v38, v38, v249\n"
    "v_mul_f32 v39, v39, v249\n"
    "v_mul_f32 v40, v40, v249\n"
    "v_mul_f32 v41, v41, v249\n"
    "v_mul_f32 v42, v42, v249\n"
    "v_mul_f32 v43, v43, v249\n"
    "v_mul_f32 v44, v44, v249\n"
    "v_mul_f32 v45, v45, v249\n"
    "v_mul_f32 v46, v46, v249\n"
    "v_mul_f32 v47, v47, v249\n"
    "v_mul_f32 v48, v48, v249\n"
    "v_mul_f32 v49, v49, v249\n"
    "v_mul_f32 v50, v50, v249\n"
    "v_mul_f32 v51, v51, v249\n"
    "v_mul_f32 v52, v52, v249\n"
    "v_mul_f32 v53, v53, v249\n"
    "v_mul_f32 v54, v54, v249\n"
    "v_mul_f32 v55, v55, v249\n"
    "v_mul_f32 v56, v56, v249\n"
    "v_mul_f32 v57, v57, v249\n"
    "v_mul_f32 v58, v58, v249\n"
    "v_mul_f32 v59, v59, v249\n"
    "v_mul_f32 v60, v60, v249\n"
    "v_mul_f32 v61, v61, v249\n"
    "v_mul_f32 v62, v62, v249\n"
    "v_mul_f32 v63, v63, v249\n"
    "v_fma_f32 v176, v144, s52, v188\n"
    "v_exp_f32 v176, v176\n"
    "v_fma_f32 v177, v145, s52, v188\n"
    "v_exp_f32 v177, v177\n"
    "v_fma_f32 v178, v146, s52, v188\n"
    "v_add_f32 v184, v176, v177\n"
    "v_cvt_pk_bf16_f32 v168, v176, v177\n"
    "v_exp_f32 v178, v178\n"
    "v_fma_f32 v179, v147, s52, v188\n"
    "v_add_f32 v184, v184, v178\n"
    "v_exp_f32 v179, v179\n"
    "v_fma_f32 v180, v148, s52, v188\n"
    "v_add_f32 v184, v184, v179\n"
    "v_cvt_pk_bf16_f32 v169, v178, v179\n"
    "v_exp_f32 v180, v180\n"
    "v_fma_f32 v181, v149, s52, v188\n"
    "v_add_f32 v184, v184, v180\n"
    "v_exp_f32 v181, v181\n"
    "v_fma_f32 v182, v150, s52, v188\n"
    "v_add_f32 v184, v184, v181\n"
    "v_cvt_pk_bf16_f32 v170, v180, v181\n"
    "v_exp_f32 v182, v182\n"
    "v_fma_f32 v183, v151, s52, v188\n"
    "v_add_f32 v184, v184, v182\n"
    "v_exp_f32 v183, v183\n"
    "v_fma_f32 v176, v152, s52, v188\n"
    "v_add_f32 v184, v184, v183\n"
    "v_cvt_pk_bf16_f32 v171, v182, v183\n"
    "v_exp_f32 v176, v176\n"
    "v_fma_f32 v177, v153, s52, v188\n"
    "v_add_f32 v184, v184, v176\n"
    "v_exp_f32 v177, v177\n"
    "v_fma_f32 v178, v154, s52, v188\n"
    "v_add_f32 v184, v184, v177\n"
    "v_cvt_pk_bf16_f32 v172, v176, v177\n"
    "v_exp_f32 v178, v178\n"
    "v_fma_f32 v179, v155, s52, v188\n"
    "v_add_f32 v184, v184, v178\n"
    "v_exp_f32 v179, v179\n"
    "v_fma_f32 v180, v156, s52, v188\n"
    "v_add_f32 v184, v184, v179\n"
    "v_cvt_pk_bf16_f32 v173, v178, v179\n"
    "v_exp_f32 v180, v180\n"
    "v_fma_f32 v181, v157, s52, v188\n"
    "v_add_f32 v184, v184, v180\n"
    "v_exp_f32 v181, v181\n"
    "v_fma_f32 v182, v158, s52, v188\n"
    "v_add_f32 v184, v184, v181\n"
    "v_exp_f32 v182, v182\n"
    "v_fma_f32 v183, v159, s52, v188\n"
    "v_add_f32 v184, v184, v182\n"
    "v_exp_f32 v183, v183\n"
    "v_cvt_pk_bf16_f32 v174, v180, v181\n"
    "v_add_f32 v184, v184, v183\n"
    "v_cvt_pk_bf16_f32 v175, v182, v183\n"
    "s_nop 3\n"
    "s_setpc_b64 s[60:61]\n"
    "rare_1_0:\n"
    "s_nop 7\n"
    "s_nop 7\n"
    "v_max3_f32 v248, v128, v129, v130\n"
    "v_max3_f32 v248, v248, v131, v132\n"
    "v_max3_f32 v248, v248, v133, v134\n"
    "v_max3_f32 v248, v248, v135, v136\n"
    "v_max3_f32 v248, v248, v137, v138\n"
    "v_max3_f32 v248, v248, v139, v140\n"
    "v_max3_f32 v248, v248, v141, v142\n"
    "v_max_f32 v248, v248, v143\n"
    "v_mov_b32 v249, v248\n"
    "s_nop 1\n"
    "v_permlane32_swap_b32 v249, v248\n"
    "v_max_f32 v248, v248, v249\n"
    "v_mul_f32_e64 v248, -s52, v248\n"
    "v_min_f32 v248, v248, v189\n"
    "v_sub_f32 v249, v248, v189\n"
    "v_exp_f32 v249, v249\n"
    "v_mov_b32 v189, v248\n"
    "v_mul_f32 v186, v186, v249\n"
    "v_mul_f32 v64, v64, v249\n"
    "v_mul_f32 v65, v65, v249\n"
    "v_mul_f32 v66, v66, v249\n"
    "v_mul_f32 v67, v67, v249\n"
    "v_mul_f32 v68, v68, v249\n"
    "v_mul_f32 v69, v69, v249\n"
    "v_mul_f32 v70, v70, v249\n"
    "v_mul_f32 v71, v71, v249\n"
    "v_mul_f32 v72, v72, v249\n"
    "v_mul_f32 v73, v73, v249\n"
    "v_mul_f32 v74, v74, v249\n"
    "v_mul_f32 v75, v75, v249\n"
    "v_mul_f32 v76, v76, v249\n"
    "v_mul_f32 v77, v77, v249\n"
    "v_mul_f32 v78, v78, v249\n"
    "v_mul_f32 v79, v79, v249\n"
    "v_mul_f32 v80, v80, v249\n"
    "v_mul_f32 v81, v81, v249\n"
    "v_mul_f32 v82, v82, v249\n"
    "v_mul_f32 v83, v83, v249\n"
    "v_mul_f32 v84, v84, v249\n"
    "v_mul_f32 v85, v85, v249\n"
    "v_mul_f32 v86, v86, v249\n"
    "v_mul_f32 v87, v87, v249\n"
    "v_mul_f32 v88, v88, v249\n"
    "v_mul_f32 v89, v89, v249\n"
    "v_mul_f32 v90, v90, v249\n"
    "v_mul_f32 v91, v91, v249\n"
    "v_mul_f32 v92, v92, v249\n"
    "v_mul_f32 v93, v93, v249\n"
    "v_mul_f32 v94, v94, v249\n"
    "v_mul_f32 v95, v95, v249\n"
    "v_mul_f32 v96, v96, v249\n"
    "v_mul_f32 v97, v97, v249\n"
    "v_mul_f32 v98, v98, v249\n"
    "v_mul_f32 v99, v99, v249\n"
    "v_mul_f32 v100, v100, v249\n"
    "v_mul_f32 v101, v101, v249\n"
    "v_mul_f32 v102, v102, v249\n"
    "v_mul_f32 v103, v103, v249\n"
    "v_mul_f32 v104, v104, v249\n"
    "v_mul_f32 v105, v105, v249\n"
    "v_mul_f32 v106, v106, v249\n"
    "v_mul_f32 v107, v107, v249\n"
    "v_mul_f32 v108, v108, v249\n"
    "v_mul_f32 v109, v109, v249\n"
    "v_mul_f32 v110, v110, v249\n"
    "v_mul_f32 v111, v111, v249\n"
    "v_mul_f32 v112, v112, v249\n"
    "v_mul_f32 v113, v113, v249\n"
    "v_mul_f32 v114, v114, v249\n"
    "v_mul_f32 v115, v115, v249\n"
    "v_mul_f32 v116, v116, v249\n"
    "v_mul_f32 v117, v117, v249\n"
    "v_mul_f32 v118, v118, v249\n"
    "v_mul_f32 v119, v119, v249\n"
    "v_mul_f32 v120, v120, v249\n"
    "v_mul_f32 v121, v121, v249\n"
    "v_mul_f32 v122, v122, v249\n"
    "v_mul_f32 v123, v123, v249\n"
    "v_mul_f32 v124, v124, v249\n"
    "v_mul_f32 v125, v125, v249\n"
    "v_mul_f32 v126, v126, v249\n"
    "v_mul_f32 v127, v127, v249\n"
    "v_fma_f32 v176, v128, s52, v189\n"
    "v_exp_f32 v176, v176\n"
    "v_fma_f32 v177, v129, s52, v189\n"
    "v_exp_f32 v177, v177\n"
    "v_fma_f32 v178, v130, s52, v189\n"
    "v_add_f32 v184, v176, v177\n"
    "v_cvt_pk_bf16_f32 v160, v176, v177\n"
    "v_exp_f32 v178, v178\n"
    "v_fma_f32 v179, v131, s52, v189\n"
    "v_add_f32 v184, v184, v178\n"
    "v_exp_f32 v179, v179\n"
    "v_fma_f32 v180, v132, s52, v189\n"
    "v_add_f32 v184, v184, v179\n"
    "v_cvt_pk_bf16_f32 v161, v178, v179\n"
    "v_exp_f32 v180, v180\n"
    "v_fma_f32 v181, v133, s52, v189\n"
    "v_add_f32 v184, v184, v180\n"
    "v_exp_f32 v181, v181\n"
    "v_fma_f32 v182, v134, s52, v189\n"
    "v_add_f32 v184, v184, v181\n"
    "v_cvt_pk_bf16_f32 v162, v180, v181\n"
    "v_exp_f32 v182, v182\n"
    "v_fma_f32 v183, v135, s52, v189\n"
    "v_add_f32 v184, v184, v182\n"
    "v_exp_f32 v183, v183\n"
    "v_fma_f32 v176, v136, s52, v189\n"
    "v_add_f32 v184, v184, v183\n"
    "v_cvt_pk_bf16_f32 v163, v182, v183\n"
    "v_exp_f32 v176, v176\n"
    "v_fma_f32 v177, v137, s52, v189\n"
    "v_add_f32 v184, v184, v176\n"
    "v_exp_f32 v177, v177\n"
    "v_fma_f32 v178, v138, s52, v189\n"
    "v_add_f32 v184, v184, v177\n"
    "v_cvt_pk_bf16_f32 v164, v176, v177\n"
    "v_exp_f32 v178, v178\n"
    "v_fma_f32 v179, v139, s52, v189\n"
    "v_add_f32 v184, v184, v178\n"
    "v_exp_f32 v179, v179\n"
    "v_fma_f32 v180, v140, s52, v189\n"
    "v_add_f32 v184, v184, v179\n"
    "v_cvt_pk_bf16_f32 v165, v178, v179\n"
    "v_exp_f32 v180, v180\n"
    "v_fma_f32 v181, v141, s52, v189\n"
    "v_add_f32 v184, v184, v180\n"
    "v_exp_f32 v181, v181\n"
    "v_fma_f32 v182, v142, s52, v189\n"
    "v_add_f32 v184, v184, v181\n"
    "v_exp_f32 v182, v182\n"
    "v_fma_f32 v183, v143, s52, v189\n"
    "v_add_f32 v184, v184, v182\n"
    "v_exp_f32 v183, v183\n"
    "v_cvt_pk_bf16_f32 v166, v180, v181\n"
    "v_add_f32 v184, v184, v183\n"
    "v_cvt_pk_bf16_f32 v167, v182, v183\n"
    "s_nop 3\n"
    "s_setpc_b64 s[60:61]\n"
    "rare_1_1:\n"
    "s_nop 7\n"
    "s_nop 7\n"
    "v_max3_f32 v248, v144, v145, v146\n"
    "v_max3_f32 v248, v248, v147, v148\n"
    "v_max3_f32 v248, v248, v149, v150\n"
    "v_max3_f32 v248, v248, v151, v152\n"
    "v_max3_f32 v248, v248, v153, v154\n"
    "v_max3_f32 v248, v248, v155, v156\n"
    "v_max3_f32 v248, v248, v157, v158\n"
    "v_max_f32 v248, v248, v159\n"
    "v_mov_b32 v249, v248\n"
    "s_nop 1\n"
    "v_permlane32_swap_b32 v249, v248\n"
    "v_max_f32 v248, v248, v249\n"
    "v_mul_f32_e64 v248, -s52, v248\n"
    "v_min_f32 v248, v248, v189\n"
    "v_sub_f32 v249, v248, v189\n"
    "v_exp_f32 v249, v249\n"
    "v_mov_b32 v189, v248\n"
    "v_mul_f32 v186, v186, v249\n"
    "v_mul_f32 v64, v64, v249\n"
    "v_mul_f32 v65, v65, v249\n"
    "v_mul_f32 v66, v66, v249\n"
    "v_mul_f32 v67, v67, v249\n"
    "v_mul_f32 v68, v68, v249\n"
    "v_mul_f32 v69, v69, v249\n"
    "v_mul_f32 v70, v70, v249\n"
    "v_mul_f32 v71, v71, v249\n"
    "v_mul_f32 v72, v72, v249\n"
    "v_mul_f32 v73, v73, v249\n"
    "v_mul_f32 v74, v74, v249\n"
    "v_mul_f32 v75, v75, v249\n"
    "v_mul_f32 v76, v76, v249\n"
    "v_mul_f32 v77, v77, v249\n"
    "v_mul_f32 v78, v78, v249\n"
    "v_mul_f32 v79, v79, v249\n"
    "v_mul_f32 v80, v80, v249\n"
    "v_mul_f32 v81, v81, v249\n"
    "v_mul_f32 v82, v82, v249\n"
    "v_mul_f32 v83, v83, v249\n"
    "v_mul_f32 v84, v84, v249\n"
    "v_mul_f32 v85, v85, v249\n"
    "v_mul_f32 v86, v86, v249\n"
    "v_mul_f32 v87, v87, v249\n"
    "v_mul_f32 v88, v88, v249\n"
    "v_mul_f32 v89, v89, v249\n"
    "v_mul_f32 v90, v90, v249\n"
    "v_mul_f32 v91, v91, v249\n"
    "v_mul_f32 v92, v92, v249\n"
    "v_mul_f32 v93, v93, v249\n"
    "v_mul_f32 v94, v94, v249\n"
    "v_mul_f32 v95, v95, v249\n"
    "v_mul_f32 v96, v96, v249\n"
    "v_mul_f32 v97, v97, v249\n"
    "v_mul_f32 v98, v98, v249\n"
    "v_mul_f32 v99, v99, v249\n"
    "v_mul_f32 v100, v100, v249\n"
    "v_mul_f32 v101, v101, v249\n"
    "v_mul_f32 v102, v102, v249\n"
    "v_mul_f32 v103, v103, v249\n"
    "v_mul_f32 v104, v104, v249\n"
    "v_mul_f32 v105, v105, v249\n"
    "v_mul_f32 v106, v106, v249\n"
    "v_mul_f32 v107, v107, v249\n"
    "v_mul_f32 v108, v108, v249\n"
    "v_mul_f32 v109, v109, v249\n"
    "v_mul_f32 v110, v110, v249\n"
    "v_mul_f32 v111, v111, v249\n"
    "v_mul_f32 v112, v112, v249\n"
    "v_mul_f32 v113, v113, v249\n"
    "v_mul_f32 v114, v114, v249\n"
    "v_mul_f32 v115, v115, v249\n"
    "v_mul_f32 v116, v116, v249\n"
    "v_mul_f32 v117, v117, v249\n"
    "v_mul_f32 v118, v118, v249\n"
    "v_mul_f32 v119, v119, v249\n"
    "v_mul_f32 v120, v120, v249\n"
    "v_mul_f32 v121, v121, v249\n"
    "v_mul_f32 v122, v122, v249\n"
    "v_mul_f32 v123, v123, v249\n"
    "v_mul_f32 v124, v124, v249\n"
    "v_mul_f32 v125, v125, v249\n"
    "v_mul_f32 v126, v126, v249\n"
    "v_mul_f32 v127, v127, v249\n"
    "v_fma_f32 v176, v144, s52, v189\n"
    "v_exp_f32 v176, v176\n"
    "v_fma_f32 v177, v145, s52, v189\n"
    "v_exp_f32 v177, v177\n"
    "v_fma_f32 v178, v146, s52, v189\n"
    "v_add_f32 v184, v176, v177\n"
    "v_cvt_pk_bf16_f32 v168, v176, v177\n"
    "v_exp_f32 v178, v178\n"
    "v_fma_f32 v179, v147, s52, v189\n"
    "v_add_f32 v184, v184, v178\n"
    "v_exp_f32 v179, v179\n"
    "v_fma_f32 v180, v148, s52, v189\n"
    "v_add_f32 v184, v184, v179\n"
    "v_cvt_pk_bf16_f32 v169, v178, v179\n"
    "v_exp_f32 v180, v180\n"
    "v_fma_f32 v181, v149, s52, v189\n"
    "v_add_f32 v184, v184, v180\n"
    "v_exp_f32 v181, v181\n"
    "v_fma_f32 v182, v150, s52, v189\n"
    "v_add_f32 v184, v184, v181\n"
    "v_cvt_pk_bf16_f32 v170, v180, v181\n"
    "v_exp_f32 v182, v182\n"
    "v_fma_f32 v183, v151, s52, v189\n"
    "v_add_f32 v184, v184, v182\n"
    "v_exp_f32 v183, v183\n"
    "v_fma_f32 v176, v152, s52, v189\n"
    "v_add_f32 v184, v184, v183\n"
    "v_cvt_pk_bf16_f32 v171, v182, v183\n"
    "v_exp_f32 v176, v176\n"
    "v_fma_f32 v177, v153, s52, v189\n"
    "v_add_f32 v184, v184, v176\n"
    "v_exp_f32 v177, v177\n"
    "v_fma_f32 v178, v154, s52, v189\n"
    "v_add_f32 v184, v184, v177\n"
    "v_cvt_pk_bf16_f32 v172, v176, v177\n"
    "v_exp_f32 v178, v178\n"
    "v_fma_f32 v179, v155, s52, v189\n"
    "v_add_f32 v184, v184, v178\n"
    "v_exp_f32 v179, v179\n"
    "v_fma_f32 v180, v156, s52, v189\n"
    "v_add_f32 v184, v184, v179\n"
    "v_cvt_pk_bf16_f32 v173, v178, v179\n"
    "v_exp_f32 v180, v180\n"
    "v_fma_f32 v181, v157, s52, v189\n"
    "v_add_f32 v184, v184, v180\n"
    "v_exp_f32 v181, v181\n"
    "v_fma_f32 v182, v158, s52, v189\n"
    "v_add_f32 v184, v184, v181\n"
    "v_exp_f32 v182, v182\n"
    "v_fma_f32 v183, v159, s52, v189\n"
    "v_add_f32 v184, v184, v182\n"
    "v_exp_f32 v183, v183\n"
    "v_cvt_pk_bf16_f32 v174, v180, v181\n"
    "v_add_f32 v184, v184, v183\n"
    "v_cvt_pk_bf16_f32 v175, v182, v183\n"
    "s_nop 3\n"
    "s_setpc_b64 s[60:61]\n"
    "rare_2_0:\n"
    "s_nop 7\n"
    "s_nop 7\n"
    "v_max3_f32 v248, v128, v129, v130\n"
    "v_max3_f32 v248, v248, v131, v132\n"
    "v_max3_f32 v248, v248, v133, v134\n"
    "v_max3_f32 v248, v248, v135, v136\n"
    "v_max3_f32 v248, v248, v137, v138\n"
    "v_max3_f32 v248, v248, v139, v140\n"
    "v_max3_f32 v248, v248, v141, v142\n"
    "v_max_f32 v248, v248, v143\n"
    "v_mov_b32 v249, v248\n"
    "s_nop 1\n"
    "v_permlane32_swap_b32 v249, v248\n"
    "v_max_f32 v248, v248, v249\n"
    "v_mul_f32_e64 v248, -s52, v248\n"
    "v_min_f32 v248, v248, v190\n"
    "v_sub_f32 v249, v248, v190\n"
    "v_exp_f32 v249, v249\n"
    "v_mov_b32 v190, v248\n"
    "v_mul_f32 v187, v187, v249\n"
    "v_accvgpr_read_b32 v250, a160\n"
    "v_mul_f32 v250, v250, v249\n"
    "v_accvgpr_write_b32 a160, v250\n"
    "v_accvgpr_read_b32 v251, a161\n"
    "v_mul_f32 v251, v251, v249\n"
    "v_accvgpr_write_b32 a161, v251\n"
    "v_accvgpr_read_b32 v250, a162\n"
    "v_mul_f32 v250, v250, v249\n"
    "v_accvgpr_write_b32 a162, v250\n"
    "v_accvgpr_read_b32 v251, a163\n"
    "v_mul_f32 v251, v251, v249\n"
    "v_accvgpr_write_b32 a163, v251\n"
    "v_accvgpr_read_b32 v250, a164\n"
    "v_mul_f32 v250, v250, v249\n"
    "v_accvgpr_write_b32 a164, v250\n"
    "v_accvgpr_read_b32 v251, a165\n"
    "v_mul_f32 v251, v251, v249\n"
    "v_accvgpr_write_b32 a165, v251\n"
    "v_accvgpr_read_b32 v250, a166\n"
    "v_mul_f32 v250, v250, v249\n"
    "v_accvgpr_write_b32 a166, v250\n"
    "v_accvgpr_read_b32 v251, a167\n"
    "v_mul_f32 v251, v251, v249\n"
    "v_accvgpr_write_b32 a167, v251\n"
    "v_accvgpr_read_b32 v250, a168\n"
    "v_mul_f32 v250, v250, v249\n"
    "v_accvgpr_write_b32 a168, v250\n"
    "v_accvgpr_read_b32 v251, a169\n"
    "v_mul_f32 v251, v251, v249\n"
    "v_accvgpr_write_b32 a169, v251\n"
    "v_accvgpr_read_b32 v250, a170\n"
    "v_mul_f32 v250, v250, v249\n"
    "v_accvgpr_write_b32 a170, v250\n"
    "v_accvgpr_read_b32 v251, a171\n"
    "v_mul_f32 v251, v251, v249\n"
    "v_accvgpr_write_b32 a171, v251\n"
    "v_accvgpr_read_b32 v250, a172\n"
    "v_mul_f32 v250, v250, v249\n"
    "v_accvgpr_write_b32 a172, v250\n"
    "v_accvgpr_read_b32 v251, a173\n"
    "v_mul_f32 v251, v251, v249\n"
    "v_accvgpr_write_b32 a173, v251\n"
    "v_accvgpr_read_b32 v250, a174\n"
    "v_mul_f32 v250, v250, v249\n"
    "v_accvgpr_write_b32 a174, v250\n"
    "v_accvgpr_read_b32 v251, a175\n"
    "v_mul_f32 v251, v251, v249\n"
    "v_accvgpr_write_b32 a175, v251\n"
    "v_accvgpr_read_b32 v250, a176\n"
    "v_mul_f32 v250, v250, v249\n"
    "v_accvgpr_write_b32 a176, v250\n"
    "v_accvgpr_read_b32 v251, a177\n"
    "v_mul_f32 v251, v251, v249\n"
    "v_accvgpr_write_b32 a177, v251\n"
    "v_accvgpr_read_b32 v250, a178\n"
    "v_mul_f32 v250, v250, v249\n"
    "v_accvgpr_write_b32 a178, v250\n"
    "v_accvgpr_read_b32 v251, a179\n"
    "v_mul_f32 v251, v251, v249\n"
    "v_accvgpr_write_b32 a179, v251\n"
    "v_accvgpr_read_b32 v250, a180\n"
    "v_mul_f32 v250, v250, v249\n"
    "v_accvgpr_write_b32 a180, v250\n"
    "v_accvgpr_read_b32 v251, a181\n"
    "v_mul_f32 v251, v251, v249\n"
    "v_accvgpr_write_b32 a181, v251\n"
    "v_accvgpr_read_b32 v250, a182\n"
    "v_mul_f32 v250, v250, v249\n"
    "v_accvgpr_write_b32 a182, v250\n"
    "v_accvgpr_read_b32 v251, a183\n"
    "v_mul_f32 v251, v251, v249\n"
    "v_accvgpr_write_b32 a183, v251\n"
    "v_accvgpr_read_b32 v250, a184\n"
    "v_mul_f32 v250, v250, v249\n"
    "v_accvgpr_write_b32 a184, v250\n"
    "v_accvgpr_read_b32 v251, a185\n"
    "v_mul_f32 v251, v251, v249\n"
    "v_accvgpr_write_b32 a185, v251\n"
    "v_accvgpr_read_b32 v250, a186\n"
    "v_mul_f32 v250, v250, v249\n"
    "v_accvgpr_write_b32 a186, v250\n"
    "v_accvgpr_read_b32 v251, a187\n"
    "v_mul_f32 v251, v251, v249\n"
    "v_accvgpr_write_b32 a187, v251\n"
    "v_accvgpr_read_b32 v250, a188\n"
    "v_mul_f32 v250, v250, v249\n"
    "v_accvgpr_write_b32 a188, v250\n"
    "v_accvgpr_read_b32 v251, a189\n"
    "v_mul_f32 v251, v251, v249\n"
    "v_accvgpr_write_b32 a189, v251\n"
    "v_accvgpr_read_b32 v250, a190\n"
    "v_mul_f32 v250, v250, v249\n"
    "v_accvgpr_write_b32 a190, v250\n"
    "v_accvgpr_read_b32 v251, a191\n"
    "v_mul_f32 v251, v251, v249\n"
    "v_accvgpr_write_b32 a191, v251\n"
    "v_accvgpr_read_b32 v250, a192\n"
    "v_mul_f32 v250, v250, v249\n"
    "v_accvgpr_write_b32 a192, v250\n"
    "v_accvgpr_read_b32 v251, a193\n"
    "v_mul_f32 v251, v251, v249\n"
    "v_accvgpr_write_b32 a193, v251\n"
    "v_accvgpr_read_b32 v250, a194\n"
    "v_mul_f32 v250, v250, v249\n"
    "v_accvgpr_write_b32 a194, v250\n"
    "v_accvgpr_read_b32 v251, a195\n"
    "v_mul_f32 v251, v251, v249\n"
    "v_accvgpr_write_b32 a195, v251\n"
    "v_accvgpr_read_b32 v250, a196\n"
    "v_mul_f32 v250, v250, v249\n"
    "v_accvgpr_write_b32 a196, v250\n"
    "v_accvgpr_read_b32 v251, a197\n"
    "v_mul_f32 v251, v251, v249\n"
    "v_accvgpr_write_b32 a197, v251\n"
    "v_accvgpr_read_b32 v250, a198\n"
    "v_mul_f32 v250, v250, v249\n"
    "v_accvgpr_write_b32 a198, v250\n"
    "v_accvgpr_read_b32 v251, a199\n"
    "v_mul_f32 v251, v251, v249\n"
    "v_accvgpr_write_b32 a199, v251\n"
    "v_accvgpr_read_b32 v250, a200\n"
    "v_mul_f32 v250, v250, v249\n"
    "v_accvgpr_write_b32 a200, v250\n"
    "v_accvgpr_read_b32 v251, a201\n"
    "v_mul_f32 v251, v251, v249\n"
    "v_accvgpr_write_b32 a201, v251\n"
    "v_accvgpr_read_b32 v250, a202\n"
    "v_mul_f32 v250, v250, v249\n"
    "v_accvgpr_write_b32 a202, v250\n"
    "v_accvgpr_read_b32 v251, a203\n"
    "v_mul_f32 v251, v251, v249\n"
    "v_accvgpr_write_b32 a203, v251\n"
    "v_accvgpr_read_b32 v250, a204\n"
    "v_mul_f32 v250, v250, v249\n"
    "v_accvgpr_write_b32 a204, v250\n"
    "v_accvgpr_read_b32 v251, a205\n"
    "v_mul_f32 v251, v251, v249\n"
    "v_accvgpr_write_b32 a205, v251\n"
    "v_accvgpr_read_b32 v250, a206\n"
    "v_mul_f32 v250, v250, v249\n"
    "v_accvgpr_write_b32 a206, v250\n"
    "v_accvgpr_read_b32 v251, a207\n"
    "v_mul_f32 v251, v251, v249\n"
    "v_accvgpr_write_b32 a207, v251\n"
    "v_accvgpr_read_b32 v250, a208\n"
    "v_mul_f32 v250, v250, v249\n"
    "v_accvgpr_write_b32 a208, v250\n"
    "v_accvgpr_read_b32 v251, a209\n"
    "v_mul_f32 v251, v251, v249\n"
    "v_accvgpr_write_b32 a209, v251\n"
    "v_accvgpr_read_b32 v250, a210\n"
    "v_mul_f32 v250, v250, v249\n"
    "v_accvgpr_write_b32 a210, v250\n"
    "v_accvgpr_read_b32 v251, a211\n"
    "v_mul_f32 v251, v251, v249\n"
    "v_accvgpr_write_b32 a211, v251\n"
    "v_accvgpr_read_b32 v250, a212\n"
    "v_mul_f32 v250, v250, v249\n"
    "v_accvgpr_write_b32 a212, v250\n"
    "v_accvgpr_read_b32 v251, a213\n"
    "v_mul_f32 v251, v251, v249\n"
    "v_accvgpr_write_b32 a213, v251\n"
    "v_accvgpr_read_b32 v250, a214\n"
    "v_mul_f32 v250, v250, v249\n"
    "v_accvgpr_write_b32 a214, v250\n"
    "v_accvgpr_read_b32 v251, a215\n"
    "v_mul_f32 v251, v251, v249\n"
    "v_accvgpr_write_b32 a215, v251\n"
    "v_accvgpr_read_b32 v250, a216\n"
    "v_mul_f32 v250, v250, v249\n"
    "v_accvgpr_write_b32 a216, v250\n"
    "v_accvgpr_read_b32 v251, a217\n"
    "v_mul_f32 v251, v251, v249\n"
    "v_accvgpr_write_b32 a217, v251\n"
    "v_accvgpr_read_b32 v250, a218\n"
    "v_mul_f32 v250, v250, v249\n"
    "v_accvgpr_write_b32 a218, v250\n"
    "v_accvgpr_read_b32 v251, a219\n"
    "v_mul_f32 v251, v251, v249\n"
    "v_accvgpr_write_b32 a219, v251\n"
    "v_accvgpr_read_b32 v250, a220\n"
    "v_mul_f32 v250, v250, v249\n"
    "v_accvgpr_write_b32 a220, v250\n"
    "v_accvgpr_read_b32 v251, a221\n"
    "v_mul_f32 v251, v251, v249\n"
    "v_accvgpr_write_b32 a221, v251\n"
    "v_accvgpr_read_b32 v250, a222\n"
    "v_mul_f32 v250, v250, v249\n"
    "v_accvgpr_write_b32 a222, v250\n"
    "v_accvgpr_read_b32 v251, a223\n"
    "v_mul_f32 v251, v251, v249\n"
    "v_accvgpr_write_b32 a223, v251\n"
    "v_fma_f32 v176, v128, s52, v190\n"
    "v_exp_f32 v176, v176\n"
    "v_fma_f32 v177, v129, s52, v190\n"
    "v_exp_f32 v177, v177\n"
    "v_fma_f32 v178, v130, s52, v190\n"
    "v_add_f32 v184, v176, v177\n"
    "v_cvt_pk_bf16_f32 v160, v176, v177\n"
    "v_exp_f32 v178, v178\n"
    "v_fma_f32 v179, v131, s52, v190\n"
    "v_add_f32 v184, v184, v178\n"
    "v_exp_f32 v179, v179\n"
    "v_fma_f32 v180, v132, s52, v190\n"
    "v_add_f32 v184, v184, v179\n"
    "v_cvt_pk_bf16_f32 v161, v178, v179\n"
    "v_exp_f32 v180, v180\n"
    "v_fma_f32 v181, v133, s52, v190\n"
    "v_add_f32 v184, v184, v180\n"
    "v_exp_f32 v181, v181\n"
    "v_fma_f32 v182, v134, s52, v190\n"
    "v_add_f32 v184, v184, v181\n"
    "v_cvt_pk_bf16_f32 v162, v180, v181\n"
    "v_exp_f32 v182, v182\n"
    "v_fma_f32 v183, v135, s52, v190\n"
    "v_add_f32 v184, v184, v182\n"
    "v_exp_f32 v183, v183\n"
    "v_fma_f32 v176, v136, s52, v190\n"
    "v_add_f32 v184, v184, v183\n"
    "v_cvt_pk_bf16_f32 v163, v182, v183\n"
    "v_exp_f32 v176, v176\n"
    "v_fma_f32 v177, v137, s52, v190\n"
    "v_add_f32 v184, v184, v176\n"
    "v_exp_f32 v177, v177\n"
    "v_fma_f32 v178, v138, s52, v190\n"
    "v_add_f32 v184, v184, v177\n"
    "v_cvt_pk_bf16_f32 v164, v176, v177\n"
    "v_exp_f32 v178, v178\n"
    "v_fma_f32 v179, v139, s52, v190\n"
    "v_add_f32 v184, v184, v178\n"
    "v_exp_f32 v179, v179\n"
    "v_fma_f32 v180, v140, s52, v190\n"
    "v_add_f32 v184, v184, v179\n"
    "v_cvt_pk_bf16_f32 v165, v178, v179\n"
    "v_exp_f32 v180, v180\n"
    "v_fma_f32 v181, v141, s52, v190\n"
    "v_add_f32 v184, v184, v180\n"
    "v_exp_f32 v181, v181\n"
    "v_fma_f32 v182, v142, s52, v190\n"
    "v_add_f32 v184, v184, v181\n"
    "v_exp_f32 v182, v182\n"
    "v_fma_f32 v183, v143, s52, v190\n"
    "v_add_f32 v184, v184, v182\n"
    "v_exp_f32 v183, v183\n"
    "v_cvt_pk_bf16_f32 v166, v180, v181\n"
    "v_add_f32 v184, v184, v183\n"
    "v_cvt_pk_bf16_f32 v167, v182, v183\n"
    "s_nop 3\n"
    "s_setpc_b64 s[60:61]\n"
    "rare_2_1:\n"
    "s_nop 7\n"
    "s_nop 7\n"
    "v_max3_f32 v248, v144, v145, v146\n"
    "v_max3_f32 v248, v248, v147, v148\n"
    "v_max3_f32 v248, v248, v149, v150\n"
    "v_max3_f32 v248, v248, v151, v152\n"
    "v_max3_f32 v248, v248, v153, v154\n"
    "v_max3_f32 v248, v248, v155, v156\n"
    "v_max3_f32 v248, v248, v157, v158\n"
    "v_max_f32 v248, v248, v159\n"
    "v_mov_b32 v249, v248\n"
    "s_nop 1\n"
    "v_permlane32_swap_b32 v249, v248\n"
    "v_max_f32 v248, v248, v249\n"
    "v_mul_f32_e64 v248, -s52, v248\n"
    "v_min_f32 v248, v248, v190\n"
    "v_sub_f32 v249, v248, v190\n"
    "v_exp_f32 v249, v249\n"
    "v_mov_b32 v190, v248\n"
    "v_mul_f32 v187, v187, v249\n"
    "v_accvgpr_read_b32 v250, a160\n"
    "v_mul_f32 v250, v250, v249\n"
    "v_accvgpr_write_b32 a160, v250\n"
    "v_accvgpr_read_b32 v251, a161\n"
    "v_mul_f32 v251, v251, v249\n"
    "v_accvgpr_write_b32 a161, v251\n"
    "v_accvgpr_read_b32 v250, a162\n"
    "v_mul_f32 v250, v250, v249\n"
    "v_accvgpr_write_b32 a162, v250\n"
    "v_accvgpr_read_b32 v251, a163\n"
    "v_mul_f32 v251, v251, v249\n"
    "v_accvgpr_write_b32 a163, v251\n"
    "v_accvgpr_read_b32 v250, a164\n"
    "v_mul_f32 v250, v250, v249\n"
    "v_accvgpr_write_b32 a164, v250\n"
    "v_accvgpr_read_b32 v251, a165\n"
    "v_mul_f32 v251, v251, v249\n"
    "v_accvgpr_write_b32 a165, v251\n"
    "v_accvgpr_read_b32 v250, a166\n"
    "v_mul_f32 v250, v250, v249\n"
    "v_accvgpr_write_b32 a166, v250\n"
    "v_accvgpr_read_b32 v251, a167\n"
    "v_mul_f32 v251, v251, v249\n"
    "v_accvgpr_write_b32 a167, v251\n"
    "v_accvgpr_read_b32 v250, a168\n"
    "v_mul_f32 v250, v250, v249\n"
    "v_accvgpr_write_b32 a168, v250\n"
    "v_accvgpr_read_b32 v251, a169\n"
    "v_mul_f32 v251, v251, v249\n"
    "v_accvgpr_write_b32 a169, v251\n"
    "v_accvgpr_read_b32 v250, a170\n"
    "v_mul_f32 v250, v250, v249\n"
    "v_accvgpr_write_b32 a170, v250\n"
    "v_accvgpr_read_b32 v251, a171\n"
    "v_mul_f32 v251, v251, v249\n"
    "v_accvgpr_write_b32 a171, v251\n"
    "v_accvgpr_read_b32 v250, a172\n"
    "v_mul_f32 v250, v250, v249\n"
    "v_accvgpr_write_b32 a172, v250\n"
    "v_accvgpr_read_b32 v251, a173\n"
    "v_mul_f32 v251, v251, v249\n"
    "v_accvgpr_write_b32 a173, v251\n"
    "v_accvgpr_read_b32 v250, a174\n"
    "v_mul_f32 v250, v250, v249\n"
    "v_accvgpr_write_b32 a174, v250\n"
    "v_accvgpr_read_b32 v251, a175\n"
    "v_mul_f32 v251, v251, v249\n"
    "v_accvgpr_write_b32 a175, v251\n"
    "v_accvgpr_read_b32 v250, a176\n"
    "v_mul_f32 v250, v250, v249\n"
    "v_accvgpr_write_b32 a176, v250\n"
    "v_accvgpr_read_b32 v251, a177\n"
    "v_mul_f32 v251, v251, v249\n"
    "v_accvgpr_write_b32 a177, v251\n"
    "v_accvgpr_read_b32 v250, a178\n"
    "v_mul_f32 v250, v250, v249\n"
    "v_accvgpr_write_b32 a178, v250\n"
    "v_accvgpr_read_b32 v251, a179\n"
    "v_mul_f32 v251, v251, v249\n"
    "v_accvgpr_write_b32 a179, v251\n"
    "v_accvgpr_read_b32 v250, a180\n"
    "v_mul_f32 v250, v250, v249\n"
    "v_accvgpr_write_b32 a180, v250\n"
    "v_accvgpr_read_b32 v251, a181\n"
    "v_mul_f32 v251, v251, v249\n"
    "v_accvgpr_write_b32 a181, v251\n"
    "v_accvgpr_read_b32 v250, a182\n"
    "v_mul_f32 v250, v250, v249\n"
    "v_accvgpr_write_b32 a182, v250\n"
    "v_accvgpr_read_b32 v251, a183\n"
    "v_mul_f32 v251, v251, v249\n"
    "v_accvgpr_write_b32 a183, v251\n"
    "v_accvgpr_read_b32 v250, a184\n"
    "v_mul_f32 v250, v250, v249\n"
    "v_accvgpr_write_b32 a184, v250\n"
    "v_accvgpr_read_b32 v251, a185\n"
    "v_mul_f32 v251, v251, v249\n"
    "v_accvgpr_write_b32 a185, v251\n"
    "v_accvgpr_read_b32 v250, a186\n"
    "v_mul_f32 v250, v250, v249\n"
    "v_accvgpr_write_b32 a186, v250\n"
    "v_accvgpr_read_b32 v251, a187\n"
    "v_mul_f32 v251, v251, v249\n"
    "v_accvgpr_write_b32 a187, v251\n"
    "v_accvgpr_read_b32 v250, a188\n"
    "v_mul_f32 v250, v250, v249\n"
    "v_accvgpr_write_b32 a188, v250\n"
    "v_accvgpr_read_b32 v251, a189\n"
    "v_mul_f32 v251, v251, v249\n"
    "v_accvgpr_write_b32 a189, v251\n"
    "v_accvgpr_read_b32 v250, a190\n"
    "v_mul_f32 v250, v250, v249\n"
    "v_accvgpr_write_b32 a190, v250\n"
    "v_accvgpr_read_b32 v251, a191\n"
    "v_mul_f32 v251, v251, v249\n"
    "v_accvgpr_write_b32 a191, v251\n"
    "v_accvgpr_read_b32 v250, a192\n"
    "v_mul_f32 v250, v250, v249\n"
    "v_accvgpr_write_b32 a192, v250\n"
    "v_accvgpr_read_b32 v251, a193\n"
    "v_mul_f32 v251, v251, v249\n"
    "v_accvgpr_write_b32 a193, v251\n"
    "v_accvgpr_read_b32 v250, a194\n"
    "v_mul_f32 v250, v250, v249\n"
    "v_accvgpr_write_b32 a194, v250\n"
    "v_accvgpr_read_b32 v251, a195\n"
    "v_mul_f32 v251, v251, v249\n"
    "v_accvgpr_write_b32 a195, v251\n"
    "v_accvgpr_read_b32 v250, a196\n"
    "v_mul_f32 v250, v250, v249\n"
    "v_accvgpr_write_b32 a196, v250\n"
    "v_accvgpr_read_b32 v251, a197\n"
    "v_mul_f32 v251, v251, v249\n"
    "v_accvgpr_write_b32 a197, v251\n"
    "v_accvgpr_read_b32 v250, a198\n"
    "v_mul_f32 v250, v250, v249\n"
    "v_accvgpr_write_b32 a198, v250\n"
    "v_accvgpr_read_b32 v251, a199\n"
    "v_mul_f32 v251, v251, v249\n"
    "v_accvgpr_write_b32 a199, v251\n"
    "v_accvgpr_read_b32 v250, a200\n"
    "v_mul_f32 v250, v250, v249\n"
    "v_accvgpr_write_b32 a200, v250\n"
    "v_accvgpr_read_b32 v251, a201\n"
    "v_mul_f32 v251, v251, v249\n"
    "v_accvgpr_write_b32 a201, v251\n"
    "v_accvgpr_read_b32 v250, a202\n"
    "v_mul_f32 v250, v250, v249\n"
    "v_accvgpr_write_b32 a202, v250\n"
    "v_accvgpr_read_b32 v251, a203\n"
    "v_mul_f32 v251, v251, v249\n"
    "v_accvgpr_write_b32 a203, v251\n"
    "v_accvgpr_read_b32 v250, a204\n"
    "v_mul_f32 v250, v250, v249\n"
    "v_accvgpr_write_b32 a204, v250\n"
    "v_accvgpr_read_b32 v251, a205\n"
    "v_mul_f32 v251, v251, v249\n"
    "v_accvgpr_write_b32 a205, v251\n"
    "v_accvgpr_read_b32 v250, a206\n"
    "v_mul_f32 v250, v250, v249\n"
    "v_accvgpr_write_b32 a206, v250\n"
    "v_accvgpr_read_b32 v251, a207\n"
    "v_mul_f32 v251, v251, v249\n"
    "v_accvgpr_write_b32 a207, v251\n"
    "v_accvgpr_read_b32 v250, a208\n"
    "v_mul_f32 v250, v250, v249\n"
    "v_accvgpr_write_b32 a208, v250\n"
    "v_accvgpr_read_b32 v251, a209\n"
    "v_mul_f32 v251, v251, v249\n"
    "v_accvgpr_write_b32 a209, v251\n"
    "v_accvgpr_read_b32 v250, a210\n"
    "v_mul_f32 v250, v250, v249\n"
    "v_accvgpr_write_b32 a210, v250\n"
    "v_accvgpr_read_b32 v251, a211\n"
    "v_mul_f32 v251, v251, v249\n"
    "v_accvgpr_write_b32 a211, v251\n"
    "v_accvgpr_read_b32 v250, a212\n"
    "v_mul_f32 v250, v250, v249\n"
    "v_accvgpr_write_b32 a212, v250\n"
    "v_accvgpr_read_b32 v251, a213\n"
    "v_mul_f32 v251, v251, v249\n"
    "v_accvgpr_write_b32 a213, v251\n"
    "v_accvgpr_read_b32 v250, a214\n"
    "v_mul_f32 v250, v250, v249\n"
    "v_accvgpr_write_b32 a214, v250\n"
    "v_accvgpr_read_b32 v251, a215\n"
    "v_mul_f32 v251, v251, v249\n"
    "v_accvgpr_write_b32 a215, v251\n"
    "v_accvgpr_read_b32 v250, a216\n"
    "v_mul_f32 v250, v250, v249\n"
    "v_accvgpr_write_b32 a216, v250\n"
    "v_accvgpr_read_b32 v251, a217\n"
    "v_mul_f32 v251, v251, v249\n"
    "v_accvgpr_write_b32 a217, v251\n"
    "v_accvgpr_read_b32 v250, a218\n"
    "v_mul_f32 v250, v250, v249\n"
    "v_accvgpr_write_b32 a218, v250\n"
    "v_accvgpr_read_b32 v251, a219\n"
    "v_mul_f32 v251, v251, v249\n"
    "v_accvgpr_write_b32 a219, v251\n"
    "v_accvgpr_read_b32 v250, a220\n"
    "v_mul_f32 v250, v250, v249\n"
    "v_accvgpr_write_b32 a220, v250\n"
    "v_accvgpr_read_b32 v251, a221\n"
    "v_mul_f32 v251, v251, v249\n"
    "v_accvgpr_write_b32 a221, v251\n"
    "v_accvgpr_read_b32 v250, a222\n"
    "v_mul_f32 v250, v250, v249\n"
    "v_accvgpr_write_b32 a222, v250\n"
    "v_accvgpr_read_b32 v251, a223\n"
    "v_mul_f32 v251, v251, v249\n"
    "v_accvgpr_write_b32 a223, v251\n"
    "v_fma_f32 v176, v144, s52, v190\n"
    "v_exp_f32 v176, v176\n"
    "v_fma_f32 v177, v145, s52, v190\n"
    "v_exp_f32 v177, v177\n"
    "v_fma_f32 v178, v146, s52, v190\n"
    "v_add_f32 v184, v176, v177\n"
    "v_cvt_pk_bf16_f32 v168, v176, v177\n"
    "v_exp_f32 v178, v178\n"
    "v_fma_f32 v179, v147, s52, v190\n"
    "v_add_f32 v184, v184, v178\n"
    "v_exp_f32 v179, v179\n"
    "v_fma_f32 v180, v148, s52, v190\n"
    "v_add_f32 v184, v184, v179\n"
    "v_cvt_pk_bf16_f32 v169, v178, v179\n"
    "v_exp_f32 v180, v180\n"
    "v_fma_f32 v181, v149, s52, v190\n"
    "v_add_f32 v184, v184, v180\n"
    "v_exp_f32 v181, v181\n"
    "v_fma_f32 v182, v150, s52, v190\n"
    "v_add_f32 v184, v184, v181\n"
    "v_cvt_pk_bf16_f32 v170, v180, v181\n"
    "v_exp_f32 v182, v182\n"
    "v_fma_f32 v183, v151, s52, v190\n"
    "v_add_f32 v184, v184, v182\n"
    "v_exp_f32 v183, v183\n"
    "v_fma_f32 v176, v152, s52, v190\n"
    "v_add_f32 v184, v184, v183\n"
    "v_cvt_pk_bf16_f32 v171, v182, v183\n"
    "v_exp_f32 v176, v176\n"
    "v_fma_f32 v177, v153, s52, v190\n"
    "v_add_f32 v184, v184, v176\n"
    "v_exp_f32 v177, v177\n"
    "v_fma_f32 v178, v154, s52, v190\n"
    "v_add_f32 v184, v184, v177\n"
    "v_cvt_pk_bf16_f32 v172, v176, v177\n"
    "v_exp_f32 v178, v178\n"
    "v_fma_f32 v179, v155, s52, v190\n"
    "v_add_f32 v184, v184, v178\n"
    "v_exp_f32 v179, v179\n"
    "v_fma_f32 v180, v156, s52, v190\n"
    "v_add_f32 v184, v184, v179\n"
    "v_cvt_pk_bf16_f32 v173, v178, v179\n"
    "v_exp_f32 v180, v180\n"
    "v_fma_f32 v181, v157, s52, v190\n"
    "v_add_f32 v184, v184, v180\n"
    "v_exp_f32 v181, v181\n"
    "v_fma_f32 v182, v158, s52, v190\n"
    "v_add_f32 v184, v184, v181\n"
    "v_exp_f32 v182, v182\n"
    "v_fma_f32 v183, v159, s52, v190\n"
    "v_add_f32 v184, v184, v182\n"
    "v_exp_f32 v183, v183\n"
    "v_cvt_pk_bf16_f32 v174, v180, v181\n"
    "v_add_f32 v184, v184, v183\n"
    "v_cvt_pk_bf16_f32 v175, v182, v183\n"
    "s_nop 3\n"
    "s_setpc_b64 s[60:61]\n"
    :
    : "s"(karg), "s"(blk), "s"(wave)
    : "v0", "v1", "v2", "v3", "v4", "v5", "v6", "v7", "v8", "v9", "v10", "v11", "v12", "v13", "v14", "v15", "v16", "v17", "v18", "v19", "v20", "v21", "v22", "v23", "v24", "v25", "v26", "v27", "v28", "v29", "v30", "v31", "v32", "v33", "v34", "v35", "v36", "v37", "v38", "v39", "v40", "v41", "v42", "v43", "v44", "v45", "v46", "v47", "v48", "v49", "v50", "v51", "v52", "v53", "v54", "v55", "v56", "v57", "v58", "v59", "v60", "v61", "v62", "v63", "v64", "v65", "v66", "v67", "v68", "v69", "v70", "v71", "v72", "v73", "v74", "v75", "v76", "v77", "v78", "v79", "v80", "v81", "v82", "v83", "v84", "v85", "v86", "v87", "v88", "v89", "v90", "v91", "v92", "v93", "v94", "v95", "v96", "v97", "v98", "v99", "v100", "v101", "v102", "v103", "v104", "v105", "v106", "v107", "v108", "v109", "v110", "v111", "v112", "v113", "v114", "v115", "v116", "v117", "v118", "v119", "v120", "v121", "v122", "v123", "v124", "v125", "v126", "v127", "v128", "v129", "v130", "v131", "v132", "v133", "v134", "v135", "v136", "v137", "v138", "v139", "v140", "v141", "v142", "v143", "v144", "v145", "v146", "v147", "v148", "v149", "v150", "v151", "v152", "v153", "v154", "v155", "v156", "v157", "v158", "v159", "v160", "v161", "v162", "v163", "v164", "v165", "v166", "v167", "v168", "v169", "v170", "v171", "v172", "v173", "v174", "v175", "v176", "v177", "v178", "v179", "v180", "v181", "v182", "v183", "v184", "v185", "v186", "v187", "v188", "v189", "v190", "v191", "v192", "v193", "v194", "v195", "v196", "v197", "v198", "v199", "v200", "v201", "v202", "v203", "v204", "v205", "v206", "v207", "v208", "v209", "v210", "v211", "v212", "v213", "v214", "v215", "v216", "v217", "v218", "v219", "v220", "v221", "v222", "v223", "v224", "v225", "v226", "v227", "v228", "v229", "v230", "v231", "v232", "v233", "v234", "v235", "v236", "v237", "v238", "v239", "v240", "v241", "v242", "v243", "v244", "v245", "v246", "v247", "v248", "v249", "v250", "v251", "v252", "v253", "v254", "v255",
      "a0", "a1", "a2", "a3", "a4", "a5", "a6", "a7", "a8", "a9", "a10", "a11", "a12", "a13", "a14", "a15", "a16", "a17", "a18", "a19", "a20", "a21", "a22", "a23", "a24", "a25", "a26", "a27", "a28", "a29", "a30", "a31", "a32", "a33", "a34", "a35", "a36", "a37", "a38", "a39", "a40", "a41", "a42", "a43", "a44", "a45", "a46", "a47", "a48", "a49", "a50", "a51", "a52", "a53", "a54", "a55", "a56", "a57", "a58", "a59", "a60", "a61", "a62", "a63", "a64", "a65", "a66", "a67", "a68", "a69", "a70", "a71", "a72", "a73", "a74", "a75", "a76", "a77", "a78", "a79", "a80", "a81", "a82", "a83", "a84", "a85", "a86", "a87", "a88", "a89", "a90", "a91", "a92", "a93", "a94", "a95", "a96", "a97", "a98", "a99", "a100", "a101", "a102", "a103", "a104", "a105", "a106", "a107", "a108", "a109", "a110", "a111", "a112", "a113", "a114", "a115", "a116", "a117", "a118", "a119", "a120", "a121", "a122", "a123", "a124", "a125", "a126", "a127", "a128", "a129", "a130", "a131", "a132", "a133", "a134", "a135", "a136", "a137", "a138", "a139", "a140", "a141", "a142", "a143", "a144", "a145", "a146", "a147", "a148", "a149", "a150", "a151", "a152", "a153", "a154", "a155", "a156", "a157", "a158", "a159", "a160", "a161", "a162", "a163", "a164", "a165", "a166", "a167", "a168", "a169", "a170", "a171", "a172", "a173", "a174", "a175", "a176", "a177", "a178", "a179", "a180", "a181", "a182", "a183", "a184", "a185", "a186", "a187", "a188", "a189", "a190", "a191", "a192", "a193", "a194", "a195", "a196", "a197", "a198", "a199", "a200", "a201", "a202", "a203", "a204", "a205", "a206", "a207", "a208", "a209", "a210", "a211", "a212", "a213", "a214", "a215", "a216", "a217", "a218", "a219", "a220", "a221", "a222", "a223", "a224", "a225", "a226", "a227", "a228", "a229", "a230", "a231", "a232", "a233", "a234", "a235", "a236", "a237", "a238", "a239", "a240", "a241", "a242", "a243", "a244", "a245", "a246", "a247", "a248", "a249", "a250", "a251", "a252", "a253", "a254", "a255",
      "s36", "s37", "s38", "s39", "s40", "s41", "s42", "s43", "s44", "s45", "s46", "s47", "s48", "s49", "s50", "s51", "s52", "s53", "s54", "s55", "s56", "s57", "s58", "s59", "s60", "s61", "s62", "s63", "s64", "s65", "s66", "s67", "s68", "s69", "s70", "s71", "s72", "s73", "s74", "s75", "s76", "s77", "s78", "s79", "s80", "s81", "s82", "s83", "s84", "s85", "s86", "s87", "s88", "s89", "vcc", "scc", "memory");
}

extern "C" void kernel_launch(void* const* d_in, const int* in_sizes, int n_in, void* d_out, int out_size, void* d_ws, size_t ws_size,
                              hipStream_t stream) {
  (void)in_sizes; (void)n_in; (void)out_size; (void)d_ws; (void)ws_size;
  (void)hipFuncSetAttribute((const void*)attn_w4x96, hipFuncAttributeMaxDynamicSharedMemorySize, 163840);
  hipLaunchKernelGGL(attn_w4x96, dim3(256), dim3(256), 163840, stream, (const void*)d_in[0], (const void*)d_in[1], (const void*)d_in[2], d_out);
}
```
